```python
import torch

def _softmax(x, axis=-1):
    return torch.softmax(x, dim=axis)
def _gelu(x):
    return torch.nn.functional.gelu(x, approximate="tanh")
def _silu(x):
    return torch.nn.functional.silu(x)
def _sigmoid(x):
    return torch.sigmoid(x)
def _layernorm(x, axis=-1, eps=1e-5):
    m = x.mean(dim=axis, keepdim=True)
    v = x.var(dim=axis, keepdim=True, unbiased=False)
    return (x - m) / torch.sqrt(v + eps)
def _scatter_add(tgt, idx, src, axis=0):
    return tgt.clone().index_add_(axis, idx, src)
def _segment_sum(src, seg, n):
    out = torch.zeros((n,) + src.shape[1:], dtype=src.dtype)
    return out.index_add_(0, seg, src)
def _pad(x, pw):
    flat = tuple(v for pair in reversed(pw) for v in pair)
    return torch.nn.functional.pad(x, flat)

kernel_name = 'standalone__mha_softmax__near_ridge'

def setup_inputs():

    _w = (lambda a: (lambda t: t.to(torch.bfloat16) if t.is_floating_point() else t)(torch.as_tensor(a)))
    B, H, Sq, Sk, D = 8, 16, 256, 512, 256
    return {
        "q": _w((lambda *s: torch.randn(*s, dtype=torch.bfloat16) * 0.8)(B, H, Sq, D)),
        "k": _w((lambda *s: torch.randn(*s, dtype=torch.bfloat16) * 0.8)(B, H, Sk, D)),
        "v": _w((lambda *s: torch.randn(*s, dtype=torch.bfloat16) * 0.05)(B, H, Sk, D)),
    }

def reference(q, k, v):
    scale = q.shape[-1] ** -0.5
    scores = torch.einsum("bhqd,bhkd->bhqk", q, k) * scale
    p = _softmax(scores, axis=-1)
    return torch.einsum("bhqk,bhkd->bhqd", p, v)
```

```cpp
#include <hip/hip_runtime.h>
#include <hip/hip_bf16.h>
typedef float    f32x16 __attribute__((ext_vector_type(16)));
typedef float    f32x2  __attribute__((ext_vector_type(2)));
typedef __bf16   bf16x8 __attribute__((ext_vector_type(8)));
typedef __bf16   bf16x4 __attribute__((ext_vector_type(4)));
typedef __bf16   bf16x2 __attribute__((ext_vector_type(2)));
typedef unsigned u32x4  __attribute__((ext_vector_type(4)));
typedef unsigned u32x2  __attribute__((ext_vector_type(2)));
#define DI __device__ __forceinline__
DI unsigned cvtpk(float lo, float hi) { f32x2 v = {lo, hi}; bf16x2 b = __builtin_convertvector(v, bf16x2); return __builtin_bit_cast(unsigned, b); }
constexpr int SQ = 256, SK = 512, D = 256, NH = 128;

__global__ __launch_bounds__(256, 1) void mha_v0(const char* __restrict__ Q, const char* __restrict__ K, const char* __restrict__ V, char* __restrict__ O) {
  __shared__ __attribute__((aligned(16))) char lds[2 * 16384];
  const int b = blockIdx.x;
  const int head = (b >> 4) * 8 + (b & 7), half = (b >> 3) & 1;
  const int tid = threadIdx.x, lane = tid & 63, w = tid >> 6, r = lane & 31, h = lane >> 5;
  const char* Qw = Q + ((size_t)head * SQ + half * 128 + w * 32 + r) * 512;
  const char* Kh = K + (size_t)head * SK * 512;
  const char* Vh = V + (size_t)head * SK * 512;

  { unsigned acc = 0;
    if (tid < 4) { acc ^= *(const unsigned*)(Kh + (size_t)tid * 65536); acc ^= *(const unsigned*)(Vh + (size_t)tid * 65536); }
    asm volatile("s_waitcnt vmcnt(0)" :: "v"(acc));
  }
  bf16x8 qf[16];
#pragma unroll
  for (int s = 0; s < 16; ++s) qf[s] = *(const bf16x8*)(Qw + 32 * s + 16 * h);
  f32x16 o[8];
#pragma unroll
  for (int i = 0; i < 8; ++i) o[i] = f32x16{};
  float m = -1e30f, l = 0.f;
  const float C = 0.0625f * 1.4426950408889634f;
  char* Kl = lds; char* Vl = lds + 16384;
  const unsigned vbase = (unsigned)(size_t)Vl;
  const int blk = (lane >> 4) & 1, q4 = (lane & 15) >> 2, p4 = lane & 3;
  for (int t = 0; t < 16; ++t) {
    __syncthreads();
#pragma unroll
    for (int i = 0; i < 4; ++i) {
      int n = tid + 256 * i, row = n >> 5, c = n & 31;
      u32x4 kv = *(const u32x4*)(Kh + (size_t)(t * 32 + row) * 512 + 16 * c);
      u32x4 vv = *(const u32x4*)(Vh + (size_t)(t * 32 + row) * 512 + 16 * c);
      *(u32x4*)(Kl + row * 512 + 16 * (c ^ (row & 15))) = kv;
      *(u32x4*)(Vl + row * 512 + 16 * (c ^ ((row & 3) << 2))) = vv;
    }
    __syncthreads();
    f32x16 s = f32x16{};
#pragma unroll
    for (int ks = 0; ks < 16; ++ks) {
      bf16x8 kf = *(const bf16x8*)(Kl + r * 512 + 16 * ((2 * ks + h) ^ (r & 15)));
      s = __builtin_amdgcn_mfma_f32_32x32x16_bf16(kf, qf[ks], s, 0, 0, 0);
    }
    float mx = s[0];
#pragma unroll
    for (int i = 1; i < 16; ++i) mx = fmaxf(mx, s[i]);
    mx = fmaxf(mx, __shfl_xor(mx, 32));
    float mn = fmaxf(m, mx);
    float alpha = __builtin_amdgcn_exp2f((m - mn) * C);
    m = mn;
    float ps = 0.f;
#pragma unroll
    for (int i = 0; i < 16; ++i) { s[i] = __builtin_amdgcn_exp2f(fmaf(s[i], C, -mn * C)); ps += s[i]; }
    ps += __shfl_xor(ps, 32);
    l = l * alpha + ps;
#pragma unroll
    for (int i = 0; i < 8; ++i)
#pragma unroll
      for (int j = 0; j < 16; ++j) o[i][j] *= alpha;
    bf16x8 pf[2];
#pragma unroll
    for (int ss = 0; ss < 2; ++ss) {
      u32x4 w4 = {cvtpk(s[8 * ss], s[8 * ss + 1]), cvtpk(s[8 * ss + 2], s[8 * ss + 3]), cvtpk(s[8 * ss + 4], s[8 * ss + 5]), cvtpk(s[8 * ss + 6], s[8 * ss + 7])};
      pf[ss] = __builtin_bit_cast(bf16x8, w4);
    }
#pragma unroll
    for (int db = 0; db < 8; ++db) {
#pragma unroll
      for (int ss = 0; ss < 2; ++ss) {
        bf16x4 lo, hi;
        unsigned a0 = vbase + (16 * ss + 4 * h + q4) * 512 + 64 * (db ^ q4) + 32 * blk + 8 * p4;
        asm volatile("ds_read_b64_tr_b16 %0, %2\n\tds_read_b64_tr_b16 %1, %2 offset:4096\n\ts_waitcnt lgkmcnt(0)" : "=&v"(lo), "=&v"(hi) : "v"(a0) : "memory");
        bf16x8 vf = __builtin_shufflevector(lo, hi, 0, 1, 2, 3, 4, 5, 6, 7);
        o[db] = __builtin_amdgcn_mfma_f32_32x32x16_bf16(vf, pf[ss], o[db], 0, 0, 0);
      }
    }
  }
  float il = 1.0f / l;
  char* Ow = O + ((size_t)head * SQ + half * 128 + w * 32 + r) * 512;
#pragma unroll
  for (int db = 0; db < 8; ++db)
#pragma unroll
    for (int g = 0; g < 4; ++g) {
      u32x2 w2 = {cvtpk(o[db][4 * g] * il, o[db][4 * g + 1] * il), cvtpk(o[db][4 * g + 2] * il, o[db][4 * g + 3] * il)};
      *(u32x2*)(Ow + 2 * (32 * db + 8 * g + 4 * h)) = w2;
    }
}

extern "C" void kernel_launch(void* const* d_in, const int* in_sizes, int n_in, void* d_out, int out_size, void* d_ws, size_t ws_size, hipStream_t stream) {
  mha_v0<<<256, 256, 0, stream>>>((const char*)d_in[0], (const char*)d_in[1], (const char*)d_in[2], (char*)d_out);
}
```

```cpp
#define PROBE 0
#include <hip/hip_runtime.h>
#include <hip/hip_bf16.h>
typedef float    f32x16 __attribute__((ext_vector_type(16)));
typedef float    f32x2  __attribute__((ext_vector_type(2)));
typedef __bf16   bf16x8 __attribute__((ext_vector_type(8)));
typedef __bf16   bf16x4 __attribute__((ext_vector_type(4)));
typedef __bf16   bf16x2 __attribute__((ext_vector_type(2)));
typedef unsigned u32x4  __attribute__((ext_vector_type(4)));
typedef unsigned u32x2  __attribute__((ext_vector_type(2)));
#define DI __device__ __forceinline__
DI unsigned cvtpk(float lo, float hi) { f32x2 v = {lo, hi}; bf16x2 b = __builtin_convertvector(v, bf16x2); return __builtin_bit_cast(unsigned, b); }
DI unsigned long long rt() { unsigned long long t; asm volatile("s_memrealtime %0\n\ts_waitcnt lgkmcnt(0)" : "=s"(t) :: "memory"); return t; }
constexpr int SQ = 256, SK = 512, D = 256, NH = 128;

__global__ __launch_bounds__(256, 1) void mha_v0(const char* __restrict__ Q, const char* __restrict__ K, const char* __restrict__ V, char* __restrict__ O) {
  __shared__ __attribute__((aligned(16))) char lds[2 * 16384];
  const unsigned long long tS = rt(); unsigned long long tX = tS;
  const int b = blockIdx.x;
  const int head = (b >> 4) * 8 + (b & 7), half = (b >> 3) & 1;
  const int tid = threadIdx.x, lane = tid & 63, w = tid >> 6, r = lane & 31, h = lane >> 5;
  const char* Qw = Q + ((size_t)head * SQ + half * 128 + w * 32 + r) * 512;
  const char* Kh = K + (size_t)head * SK * 512;
  const char* Vh = V + (size_t)head * SK * 512;
  bf16x8 qf[16];
#pragma unroll
  for (int s = 0; s < 16; ++s) qf[s] = *(const bf16x8*)(Qw + 32 * s + 16 * h);
  f32x16 o[8];
#pragma unroll
  for (int i = 0; i < 8; ++i) o[i] = f32x16{};
  float m = -1e30f, l = 0.f;
  const float C = 0.0625f * 1.4426950408889634f;
  char* Kl = lds; char* Vl = lds + 16384;
  const unsigned vbase = (unsigned)(size_t)Vl;
  const int blk = (lane >> 4) & 1, q4 = (lane & 15) >> 2, p4 = lane & 3;
  for (int t = 0; t < 16; ++t) {
    __syncthreads();
#pragma unroll
    for (int i = 0; i < 4; ++i) {
      int n = tid + 256 * i, row = n >> 5, c = n & 31;
      u32x4 kv = *(const u32x4*)(Kh + (size_t)(t * 32 + row) * 512 + 16 * c);
      u32x4 vv = *(const u32x4*)(Vh + (size_t)(t * 32 + row) * 512 + 16 * c);
      *(u32x4*)(Kl + row * 512 + 16 * (c ^ (row & 15))) = kv;
      *(u32x4*)(Vl + row * 512 + 16 * (c ^ ((row & 3) << 2))) = vv;
    }
    __syncthreads();
    if (PROBE == 2 && t == 0) tX = rt();
    if (PROBE == 4 && t == 1) tX = rt();
    if (PROBE == 5 && t == 8) tX = rt();
    f32x16 s = f32x16{};
#pragma unroll
    for (int ks = 0; ks < 16; ++ks) {
      bf16x8 kf = *(const bf16x8*)(Kl + r * 512 + 16 * ((2 * ks + h) ^ (r & 15)));
      s = __builtin_amdgcn_mfma_f32_32x32x16_bf16(kf, qf[ks], s, 0, 0, 0);
    }
    float mx = s[0];
#pragma unroll
    for (int i = 1; i < 16; ++i) mx = fmaxf(mx, s[i]);
    mx = fmaxf(mx, __shfl_xor(mx, 32));
    float mn = fmaxf(m, mx);
    float alpha = __builtin_amdgcn_exp2f((m - mn) * C);
    m = mn;
    float ps = 0.f;
#pragma unroll
    for (int i = 0; i < 16; ++i) { s[i] = __builtin_amdgcn_exp2f(fmaf(s[i], C, -mn * C)); ps += s[i]; }
    ps += __shfl_xor(ps, 32);
    l = l * alpha + ps;
#pragma unroll
    for (int i = 0; i < 8; ++i)
#pragma unroll
      for (int j = 0; j < 16; ++j) o[i][j] *= alpha;
    bf16x8 pf[2];
#pragma unroll
    for (int ss = 0; ss < 2; ++ss) {
      u32x4 w4 = {cvtpk(s[8 * ss], s[8 * ss + 1]), cvtpk(s[8 * ss + 2], s[8 * ss + 3]), cvtpk(s[8 * ss + 4], s[8 * ss + 5]), cvtpk(s[8 * ss + 6], s[8 * ss + 7])};
      pf[ss] = __builtin_bit_cast(bf16x8, w4);
    }
#pragma unroll
    for (int db = 0; db < 8; ++db) {
#pragma unroll
      for (int ss = 0; ss < 2; ++ss) {
        bf16x4 lo, hi;
        unsigned a0 = vbase + (16 * ss + 4 * h + q4) * 512 + 64 * (db ^ q4) + 32 * blk + 8 * p4;
        asm volatile("ds_read_b64_tr_b16 %0, %2\n\tds_read_b64_tr_b16 %1, %2 offset:4096\n\ts_waitcnt lgkmcnt(0)" : "=&v"(lo), "=&v"(hi) : "v"(a0) : "memory");
        bf16x8 vf = __builtin_shufflevector(lo, hi, 0, 1, 2, 3, 4, 5, 6, 7);
        o[db] = __builtin_amdgcn_mfma_f32_32x32x16_bf16(vf, pf[ss], o[db], 0, 0, 0);
      }
    }
  }
  if (PROBE == 1) tX = rt();
  float il = 1.0f / l;
  char* Ow = O + ((size_t)head * SQ + half * 128 + w * 32 + r) * 512;
#pragma unroll
  for (int db = 0; db < 8; ++db)
#pragma unroll
    for (int g = 0; g < 4; ++g) {
      u32x2 w2 = {cvtpk(o[db][4 * g] * il, o[db][4 * g + 1] * il), cvtpk(o[db][4 * g + 2] * il, o[db][4 * g + 3] * il)};
      *(u32x2*)(Ow + 2 * (32 * db + 8 * g + 4 * h)) = w2;
    }

  asm volatile("s_waitcnt vmcnt(0)" ::: "memory");
  if (PROBE == 3) tX = rt();
  { unsigned long long tgt = tS + 10000 + (tX - tS); while (rt() < tgt) __builtin_amdgcn_s_sleep(2); }
}

extern "C" void kernel_launch(void* const* d_in, const int* in_sizes, int n_in, void* d_out, int out_size, void* d_ws, size_t ws_size, hipStream_t stream) {
  mha_v0<<<256, 256, 0, stream>>>((const char*)d_in[0], (const char*)d_in[1], (const char*)d_in[2], (char*)d_out);
}
```

```cpp
#define STPOL "sc1"
#define PROBE 0
#include <hip/hip_runtime.h>
#include <hip/hip_bf16.h>
typedef float    f32x16 __attribute__((ext_vector_type(16)));
typedef float    f32x2  __attribute__((ext_vector_type(2)));
typedef __bf16   bf16x8 __attribute__((ext_vector_type(8)));
typedef __bf16   bf16x4 __attribute__((ext_vector_type(4)));
typedef __bf16   bf16x2 __attribute__((ext_vector_type(2)));
typedef unsigned u32x4  __attribute__((ext_vector_type(4)));
typedef unsigned u32x2  __attribute__((ext_vector_type(2)));
#define DI __device__ __forceinline__
DI unsigned cvtpk(float lo, float hi) { f32x2 v = {lo, hi}; bf16x2 b = __builtin_convertvector(v, bf16x2); return __builtin_bit_cast(unsigned, b); }
DI unsigned long long rt() { unsigned long long t; asm volatile("s_memrealtime %0\n\ts_waitcnt lgkmcnt(0)" : "=s"(t) :: "memory"); return t; }
constexpr int SQ = 256, SK = 512, D = 256, NH = 128;

__global__ __launch_bounds__(256, 1) void mha_v0(const char* __restrict__ Q, const char* __restrict__ K, const char* __restrict__ V, char* __restrict__ O) {
  __shared__ __attribute__((aligned(16))) char lds[4 * 16384];
  const unsigned long long tS = rt(); unsigned long long tX = tS;
  const int b = blockIdx.x;
  const int head = (b >> 4) * 8 + (b & 7), half = (b >> 3) & 1;
  const int tid = threadIdx.x, lane = tid & 63, w = tid >> 6, r = lane & 31, h = lane >> 5;
  const char* Qw = Q + ((size_t)head * SQ + half * 128 + w * 32 + r) * 512;
  const char* Kh = K + (size_t)head * SK * 512;
  const char* Vh = V + (size_t)head * SK * 512;
  bf16x8 qf[16];
#pragma unroll
  for (int s = 0; s < 16; ++s) qf[s] = *(const bf16x8*)(Qw + 32 * s + 16 * h);
  f32x16 o[8];
#pragma unroll
  for (int i = 0; i < 8; ++i) o[i] = f32x16{};
  float m = -1e30f, l = 0.f;
  const float C = 0.0625f * 1.4426950408889634f;
  char* Kl = lds; char* Vl = lds + 16384;
  const unsigned vbase = (unsigned)(size_t)Vl;
  const int blk = (lane >> 4) & 1, q4 = (lane & 15) >> 2, p4 = lane & 3;
  for (int t = 0; t < 16; ++t) {
    __syncthreads();
#pragma unroll
    for (int i = 0; i < 4; ++i) {
      int n = tid + 256 * i, row = n >> 5, c = n & 31;
      u32x4 kv = *(const u32x4*)(Kh + (size_t)(t * 32 + row) * 512 + 16 * c);
      u32x4 vv = *(const u32x4*)(Vh + (size_t)(t * 32 + row) * 512 + 16 * c);
      *(u32x4*)(Kl + row * 512 + 16 * (c ^ (row & 15))) = kv;
      *(u32x4*)(Vl + row * 512 + 16 * (c ^ ((row & 3) << 2))) = vv;
    }
    __syncthreads();
    if (PROBE == 2 && t == 0) tX = rt();
    if (PROBE == 4 && t == 1) tX = rt();
    if (PROBE == 5 && t == 8) tX = rt();
    f32x16 s = f32x16{};
#pragma unroll
    for (int ks = 0; ks < 16; ++ks) {
      bf16x8 kf = *(const bf16x8*)(Kl + r * 512 + 16 * ((2 * ks + h) ^ (r & 15)));
      s = __builtin_amdgcn_mfma_f32_32x32x16_bf16(kf, qf[ks], s, 0, 0, 0);
    }
    float mx = s[0];
#pragma unroll
    for (int i = 1; i < 16; ++i) mx = fmaxf(mx, s[i]);
    mx = fmaxf(mx, __shfl_xor(mx, 32));
    float mn = fmaxf(m, mx);
    float alpha = __builtin_amdgcn_exp2f((m - mn) * C);
    m = mn;
    float ps = 0.f;
#pragma unroll
    for (int i = 0; i < 16; ++i) { s[i] = __builtin_amdgcn_exp2f(fmaf(s[i], C, -mn * C)); ps += s[i]; }
    ps += __shfl_xor(ps, 32);
    l = l * alpha + ps;
#pragma unroll
    for (int i = 0; i < 8; ++i)
#pragma unroll
      for (int j = 0; j < 16; ++j) o[i][j] *= alpha;
    bf16x8 pf[2];
#pragma unroll
    for (int ss = 0; ss < 2; ++ss) {
      u32x4 w4 = {cvtpk(s[8 * ss], s[8 * ss + 1]), cvtpk(s[8 * ss + 2], s[8 * ss + 3]), cvtpk(s[8 * ss + 4], s[8 * ss + 5]), cvtpk(s[8 * ss + 6], s[8 * ss + 7])};
      pf[ss] = __builtin_bit_cast(bf16x8, w4);
    }
#pragma unroll
    for (int db = 0; db < 8; ++db) {
#pragma unroll
      for (int ss = 0; ss < 2; ++ss) {
        bf16x4 lo, hi;
        unsigned a0 = vbase + (16 * ss + 4 * h + q4) * 512 + 64 * (db ^ q4) + 32 * blk + 8 * p4;
        asm volatile("ds_read_b64_tr_b16 %0, %2\n\tds_read_b64_tr_b16 %1, %2 offset:4096\n\ts_waitcnt lgkmcnt(0)" : "=&v"(lo), "=&v"(hi) : "v"(a0) : "memory");
        bf16x8 vf = __builtin_shufflevector(lo, hi, 0, 1, 2, 3, 4, 5, 6, 7);
        o[db] = __builtin_amdgcn_mfma_f32_32x32x16_bf16(vf, pf[ss], o[db], 0, 0, 0);
      }
    }
  }
  if (PROBE == 1) tX = rt();
  float il = 1.0f / l;
  __syncthreads();
  char* stg = lds + w * 16384;
#pragma unroll
  for (int db = 0; db < 8; ++db)
#pragma unroll
    for (int g = 0; g < 4; ++g) {
      u32x2 w2 = {cvtpk(o[db][4 * g] * il, o[db][4 * g + 1] * il), cvtpk(o[db][4 * g + 2] * il, o[db][4 * g + 3] * il)};
      int c = 4 * db + g;
      *(u32x2*)(stg + r * 512 + 16 * (c ^ (r & 15)) + 8 * h) = w2;
    }
  __syncthreads();
  char* Ow = O + ((size_t)head * SQ + half * 128 + w * 32) * 512 + lane * 16;
#pragma unroll
  for (int i = 0; i < 16; ++i) {
    int row = 2 * i + h, c = lane & 31;
    u32x4 val = *(const u32x4*)(stg + row * 512 + 16 * (c ^ (row & 15)));
    asm volatile("global_store_dwordx4 %0, %1, off " STPOL "\n\ts_nop 1" :: "v"(Ow + i * 1024), "v"(val) : "memory");
  }
  asm volatile("s_waitcnt vmcnt(0)" ::: "memory");
  if (PROBE == 3) tX = rt();

}

__global__ void spin_k() { unsigned long long tgt = rt() + 10000; while (rt() < tgt) __builtin_amdgcn_s_sleep(2); }
extern "C" void kernel_launch(void* const* d_in, const int* in_sizes, int n_in, void* d_out, int out_size, void* d_ws, size_t ws_size, hipStream_t stream) {
  mha_v0<<<256, 256, 0, stream>>>((const char*)d_in[0], (const char*)d_in[1], (const char*)d_in[2], (char*)d_out);
}
```

```cpp
#define LDPOL "nt"
#define STPOL2 "sc1"
#define STPOL "sc1"
#define PROBE 0
#include <hip/hip_runtime.h>
#include <hip/hip_bf16.h>
typedef float    f32x16 __attribute__((ext_vector_type(16)));
typedef float    f32x2  __attribute__((ext_vector_type(2)));
typedef __bf16   bf16x8 __attribute__((ext_vector_type(8)));
typedef __bf16   bf16x4 __attribute__((ext_vector_type(4)));
typedef __bf16   bf16x2 __attribute__((ext_vector_type(2)));
typedef unsigned u32x4  __attribute__((ext_vector_type(4)));
typedef unsigned u32x2  __attribute__((ext_vector_type(2)));
#define DI __device__ __forceinline__
DI unsigned cvtpk(float lo, float hi) { f32x2 v = {lo, hi}; bf16x2 b = __builtin_convertvector(v, bf16x2); return __builtin_bit_cast(unsigned, b); }
DI unsigned long long rt() { unsigned long long t; asm volatile("s_memrealtime %0\n\ts_waitcnt lgkmcnt(0)" : "=s"(t) :: "memory"); return t; }
constexpr int SQ = 256, SK = 512, D = 256, NH = 128;

__global__ __launch_bounds__(256, 1) void mha_v0(const char* __restrict__ Q, const char* __restrict__ K, const char* __restrict__ V, char* __restrict__ O) {
  __shared__ __attribute__((aligned(16))) char lds[4 * 16384];
  const unsigned long long tS = rt(); unsigned long long tX = tS;
  const int b = blockIdx.x;
  const int head = (b >> 4) * 8 + (b & 7), half = (b >> 3) & 1;
  const int tid = threadIdx.x, lane = tid & 63, w = tid >> 6, r = lane & 31, h = lane >> 5;
  const char* Qw = Q + ((size_t)head * SQ + half * 128 + w * 32 + r) * 512;
  const char* Kh = K + (size_t)head * SK * 512;
  const char* Vh = V + (size_t)head * SK * 512;

  { const int wv = __builtin_amdgcn_readfirstlane(tid >> 6);
    const char* Qh = Q + ((size_t)head * 256 + half * 128) * 512;
    char* Ow0 = O + ((size_t)head * SQ + half * 128 + wv * 32) * 512 + lane * 16;
    const unsigned ldsb = (unsigned)(size_t)lds;
    u32x4 val = {1,2,3,(unsigned)tid};
    auto issue = [&](int t) {
      const char* src = t < 4 ? Qh + t * 16384 : (t < 20 ? Kh + (t - 4) * 16384 : Vh + (t - 20) * 16384);
      src += wv * 4096 + lane * 16;
      unsigned dst = ldsb + (t % 4) * 16384 + wv * 4096;
#pragma unroll
      for (int i = 0; i < 4; ++i)
        asm volatile("s_mov_b32 m0, %0\n\ts_nop 0\n\tglobal_load_lds_dwordx4 %1, off " LDPOL :: "s"(dst + i * 1024), "v"(src + i * 1024) : "memory");
    };
    auto st = [&](int i) { asm volatile("global_store_dwordx4 %0, %1, off " STPOL2 "\n\ts_nop 1" :: "v"(Ow0 + i * 1024), "v"(val) : "memory"); };
    issue(0);
    issue(1);
    issue(2);
    issue(3);
    asm volatile("s_waitcnt vmcnt(12)\n\ts_barrier" ::: "memory");
    issue(4);
    asm volatile("s_waitcnt vmcnt(12)\n\ts_barrier" ::: "memory");
    issue(5);
    asm volatile("s_waitcnt vmcnt(12)\n\ts_barrier" ::: "memory");
    issue(6);
    asm volatile("s_waitcnt vmcnt(12)\n\ts_barrier" ::: "memory");
    issue(7);
    asm volatile("s_waitcnt vmcnt(12)\n\ts_barrier" ::: "memory");
    issue(8);
    asm volatile("s_waitcnt vmcnt(12)\n\ts_barrier" ::: "memory");
    issue(9);
    asm volatile("s_waitcnt vmcnt(12)\n\ts_barrier" ::: "memory");
    issue(10);
    asm volatile("s_waitcnt vmcnt(12)\n\ts_barrier" ::: "memory");
    issue(11);
    asm volatile("s_waitcnt vmcnt(12)\n\ts_barrier" ::: "memory");
    issue(12);
    asm volatile("s_waitcnt vmcnt(12)\n\ts_barrier" ::: "memory");
    issue(13);
    asm volatile("s_waitcnt vmcnt(12)\n\ts_barrier" ::: "memory");
    issue(14);
    asm volatile("s_waitcnt vmcnt(12)\n\ts_barrier" ::: "memory");
    issue(15);
    asm volatile("s_waitcnt vmcnt(12)\n\ts_barrier" ::: "memory");
    issue(16);
    asm volatile("s_waitcnt vmcnt(12)\n\ts_barrier" ::: "memory");
    issue(17);
    asm volatile("s_waitcnt vmcnt(12)\n\ts_barrier" ::: "memory");
    issue(18);
    asm volatile("s_waitcnt vmcnt(12)\n\ts_barrier" ::: "memory");
    issue(19);
    asm volatile("s_waitcnt vmcnt(12)\n\ts_barrier" ::: "memory");
    issue(20);
    asm volatile("s_waitcnt vmcnt(12)\n\ts_barrier" ::: "memory");
    issue(21);
    asm volatile("s_waitcnt vmcnt(12)\n\ts_barrier" ::: "memory");
    issue(22);
    asm volatile("s_waitcnt vmcnt(12)\n\ts_barrier" ::: "memory");
    issue(23);
    asm volatile("s_waitcnt vmcnt(12)\n\ts_barrier" ::: "memory");
    issue(24);
    asm volatile("s_waitcnt vmcnt(12)\n\ts_barrier" ::: "memory");
    issue(25);
    asm volatile("s_waitcnt vmcnt(12)\n\ts_barrier" ::: "memory");
    issue(26);
    asm volatile("s_waitcnt vmcnt(12)\n\ts_barrier" ::: "memory");
    issue(27);
    asm volatile("s_waitcnt vmcnt(12)\n\ts_barrier" ::: "memory");
    issue(28);
    st(0);
    st(1);
    st(2);
    st(3);
    asm volatile("s_waitcnt vmcnt(16)\n\ts_barrier" ::: "memory");
    issue(29);
    asm volatile("s_waitcnt vmcnt(16)\n\ts_barrier" ::: "memory");
    issue(30);
    asm volatile("s_waitcnt vmcnt(16)\n\ts_barrier" ::: "memory");
    issue(31);
    asm volatile("s_waitcnt vmcnt(16)\n\ts_barrier" ::: "memory");
    issue(32);
    st(4);
    st(5);
    st(6);
    st(7);
    asm volatile("s_waitcnt vmcnt(16)\n\ts_barrier" ::: "memory");
    issue(33);
    asm volatile("s_waitcnt vmcnt(16)\n\ts_barrier" ::: "memory");
    issue(34);
    asm volatile("s_waitcnt vmcnt(16)\n\ts_barrier" ::: "memory");
    issue(35);
    asm volatile("s_waitcnt vmcnt(16)\n\ts_barrier" ::: "memory");
    st(8);
    st(9);
    st(10);
    st(11);
    asm volatile("s_waitcnt vmcnt(12)\n\ts_barrier" ::: "memory");
    asm volatile("s_waitcnt vmcnt(8)\n\ts_barrier" ::: "memory");
    asm volatile("s_waitcnt vmcnt(4)\n\ts_barrier" ::: "memory");
    st(12);
    st(13);
    st(14);
    st(15);
    asm volatile("s_waitcnt vmcnt(0)\n\ts_barrier" ::: "memory");
    tX = rt();
  }
  bf16x8 qf[16];
#pragma unroll
  for (int s = 0; s < 16; ++s) qf[s] = *(const bf16x8*)(Qw + 32 * s + 16 * h);
  f32x16 o[8];
#pragma unroll
  for (int i = 0; i < 8; ++i) o[i] = f32x16{};
  float m = -1e30f, l = 0.f;
  const float C = 0.0625f * 1.4426950408889634f;
  char* Kl = lds; char* Vl = lds + 16384;
  const unsigned vbase = (unsigned)(size_t)Vl;
  const int blk = (lane >> 4) & 1, q4 = (lane & 15) >> 2, p4 = lane & 3;
  for (int t = 0; t < 16; ++t) {
    __syncthreads();
#pragma unroll
    for (int i = 0; i < 4; ++i) {
      int n = tid + 256 * i, row = n >> 5, c = n & 31;
      u32x4 kv = *(const u32x4*)(Kh + (size_t)(t * 32 + row) * 512 + 16 * c);
      u32x4 vv = *(const u32x4*)(Vh + (size_t)(t * 32 + row) * 512 + 16 * c);
      *(u32x4*)(Kl + row * 512 + 16 * (c ^ (row & 15))) = kv;
      *(u32x4*)(Vl + row * 512 + 16 * (c ^ ((row & 3) << 2))) = vv;
    }
    __syncthreads();
    if (PROBE == 2 && t == 0) tX = rt();
    if (PROBE == 4 && t == 1) tX = rt();
    if (PROBE == 5 && t == 8) tX = rt();
    f32x16 s = f32x16{};
#pragma unroll
    for (int ks = 0; ks < 16; ++ks) {
      bf16x8 kf = *(const bf16x8*)(Kl + r * 512 + 16 * ((2 * ks + h) ^ (r & 15)));
      s = __builtin_amdgcn_mfma_f32_32x32x16_bf16(kf, qf[ks], s, 0, 0, 0);
    }
    float mx = s[0];
#pragma unroll
    for (int i = 1; i < 16; ++i) mx = fmaxf(mx, s[i]);
    mx = fmaxf(mx, __shfl_xor(mx, 32));
    float mn = fmaxf(m, mx);
    float alpha = __builtin_amdgcn_exp2f((m - mn) * C);
    m = mn;
    float ps = 0.f;
#pragma unroll
    for (int i = 0; i < 16; ++i) { s[i] = __builtin_amdgcn_exp2f(fmaf(s[i], C, -mn * C)); ps += s[i]; }
    ps += __shfl_xor(ps, 32);
    l = l * alpha + ps;
#pragma unroll
    for (int i = 0; i < 8; ++i)
#pragma unroll
      for (int j = 0; j < 16; ++j) o[i][j] *= alpha;
    bf16x8 pf[2];
#pragma unroll
    for (int ss = 0; ss < 2; ++ss) {
      u32x4 w4 = {cvtpk(s[8 * ss], s[8 * ss + 1]), cvtpk(s[8 * ss + 2], s[8 * ss + 3]), cvtpk(s[8 * ss + 4], s[8 * ss + 5]), cvtpk(s[8 * ss + 6], s[8 * ss + 7])};
      pf[ss] = __builtin_bit_cast(bf16x8, w4);
    }
#pragma unroll
    for (int db = 0; db < 8; ++db) {
#pragma unroll
      for (int ss = 0; ss < 2; ++ss) {
        bf16x4 lo, hi;
        unsigned a0 = vbase + (16 * ss + 4 * h + q4) * 512 + 64 * (db ^ q4) + 32 * blk + 8 * p4;
        asm volatile("ds_read_b64_tr_b16 %0, %2\n\tds_read_b64_tr_b16 %1, %2 offset:4096\n\ts_waitcnt lgkmcnt(0)" : "=&v"(lo), "=&v"(hi) : "v"(a0) : "memory");
        bf16x8 vf = __builtin_shufflevector(lo, hi, 0, 1, 2, 3, 4, 5, 6, 7);
        o[db] = __builtin_amdgcn_mfma_f32_32x32x16_bf16(vf, pf[ss], o[db], 0, 0, 0);
      }
    }
  }
  if (PROBE == 1) tX = rt();
  float il = 1.0f / l;
  __syncthreads();
  char* stg = lds + w * 16384;
#pragma unroll
  for (int db = 0; db < 8; ++db)
#pragma unroll
    for (int g = 0; g < 4; ++g) {
      u32x2 w2 = {cvtpk(o[db][4 * g] * il, o[db][4 * g + 1] * il), cvtpk(o[db][4 * g + 2] * il, o[db][4 * g + 3] * il)};
      int c = 4 * db + g;
      *(u32x2*)(stg + r * 512 + 16 * (c ^ (r & 15)) + 8 * h) = w2;
    }
  __syncthreads();
  char* Ow = O + ((size_t)head * SQ + half * 128 + w * 32) * 512 + lane * 16;
#pragma unroll
  for (int i = 0; i < 16; ++i) {
    int row = 2 * i + h, c = lane & 31;
    u32x4 val = *(const u32x4*)(stg + row * 512 + 16 * (c ^ (row & 15)));
    asm volatile("global_store_dwordx4 %0, %1, off " STPOL "\n\ts_nop 1" :: "v"(Ow + i * 1024), "v"(val) : "memory");
  }
  asm volatile("s_waitcnt vmcnt(0)" ::: "memory");
  if (PROBE == 3) tX = rt();
  { unsigned long long tgt = tS + 10000 + (tX - tS); while (rt() < tgt) __builtin_amdgcn_s_sleep(2); }
}

extern "C" void kernel_launch(void* const* d_in, const int* in_sizes, int n_in, void* d_out, int out_size, void* d_ws, size_t ws_size, hipStream_t stream) {
  mha_v0<<<256, 256, 0, stream>>>((const char*)d_in[0], (const char*)d_in[1], (const char*)d_in[2], (char*)d_out);
}
```

```cpp
#define LDPOL "nt"
#define STPOL2 ""
#define STPOL "sc1"
#define PROBE 0
#include <hip/hip_runtime.h>
#include <hip/hip_bf16.h>
typedef float    f32x16 __attribute__((ext_vector_type(16)));
typedef float    f32x2  __attribute__((ext_vector_type(2)));
typedef __bf16   bf16x8 __attribute__((ext_vector_type(8)));
typedef __bf16   bf16x4 __attribute__((ext_vector_type(4)));
typedef __bf16   bf16x2 __attribute__((ext_vector_type(2)));
typedef unsigned u32x4  __attribute__((ext_vector_type(4)));
typedef unsigned u32x2  __attribute__((ext_vector_type(2)));
#define DI __device__ __forceinline__
DI unsigned cvtpk(float lo, float hi) { f32x2 v = {lo, hi}; bf16x2 b = __builtin_convertvector(v, bf16x2); return __builtin_bit_cast(unsigned, b); }
DI unsigned long long rt() { unsigned long long t; asm volatile("s_memrealtime %0\n\ts_waitcnt lgkmcnt(0)" : "=s"(t) :: "memory"); return t; }
constexpr int SQ = 256, SK = 512, D = 256, NH = 128;

__global__ __launch_bounds__(256, 1) void mha_v0(const char* __restrict__ Q, const char* __restrict__ K, const char* __restrict__ V, char* __restrict__ O) {
  __shared__ __attribute__((aligned(16))) char lds[4 * 16384];
  const unsigned long long tS = rt(); unsigned long long tX = tS;
  const int b = blockIdx.x;
  const int head = (b >> 4) * 8 + (b & 7), half = (b >> 3) & 1;
  const int tid = threadIdx.x, lane = tid & 63, w = tid >> 6, r = lane & 31, h = lane >> 5;
  const char* Qw = Q + ((size_t)head * SQ + half * 128 + w * 32 + r) * 512;
  const char* Kh = K + (size_t)head * SK * 512;
  const char* Vh = V + (size_t)head * SK * 512;

  unsigned tdummy;
  { const int wv = __builtin_amdgcn_readfirstlane(tid >> 6);
    const char* Qh = Q + ((size_t)head * 256 + half * 128) * 512;
    char* Ow0 = O + ((size_t)head * SQ + half * 128 + wv * 32) * 512 + lane * 16;
    const unsigned ldsb = (unsigned)(size_t)lds;
    u32x4 val = {1,2,3,(unsigned)tid};
    auto issue = [&](int t) {
      const char* src = t < 4 ? Qh + t * 16384 : (t < 20 ? Kh + (t - 4) * 16384 : Vh + (t - 20) * 16384);
      src += wv * 4096 + lane * 16;
      unsigned dst = ldsb + (t % 4) * 16384 + wv * 4096;
#pragma unroll
      for (int i = 0; i < 4; ++i)
        asm volatile("s_mov_b32 m0, %0\n\ts_nop 0\n\tglobal_load_lds_dwordx4 %1, off " LDPOL :: "s"(dst + i * 1024), "v"(src + i * 1024) : "memory");
    };
    auto st = [&](int i) { asm volatile("global_store_dwordx4 %0, %1, off " STPOL2 "\n\ts_nop 1" :: "v"(Ow0 + i * 1024), "v"(val) : "memory"); };
    { unsigned dummy; const char* op = O + ((size_t)head * SQ + half * 128) * 512 + (lane & 15) * 4096 + wv * 1024;
      asm volatile("global_load_dword %0, %1, off" : "=v"(dummy) : "v"(op) : "memory"); tdummy = dummy; }
    issue(0);
    issue(1);
    issue(2);
    issue(3);
    asm volatile("s_waitcnt vmcnt(12)\n\ts_barrier" ::: "memory");
    issue(4);
    asm volatile("s_waitcnt vmcnt(12)\n\ts_barrier" ::: "memory");
    issue(5);
    asm volatile("s_waitcnt vmcnt(12)\n\ts_barrier" ::: "memory");
    issue(6);
    asm volatile("s_waitcnt vmcnt(12)\n\ts_barrier" ::: "memory");
    issue(7);
    asm volatile("s_waitcnt vmcnt(12)\n\ts_barrier" ::: "memory");
    issue(8);
    asm volatile("s_waitcnt vmcnt(12)\n\ts_barrier" ::: "memory");
    issue(9);
    asm volatile("s_waitcnt vmcnt(12)\n\ts_barrier" ::: "memory");
    issue(10);
    asm volatile("s_waitcnt vmcnt(12)\n\ts_barrier" ::: "memory");
    issue(11);
    asm volatile("s_waitcnt vmcnt(12)\n\ts_barrier" ::: "memory");
    issue(12);
    asm volatile("s_waitcnt vmcnt(12)\n\ts_barrier" ::: "memory");
    issue(13);
    asm volatile("s_waitcnt vmcnt(12)\n\ts_barrier" ::: "memory");
    issue(14);
    asm volatile("s_waitcnt vmcnt(12)\n\ts_barrier" ::: "memory");
    issue(15);
    asm volatile("s_waitcnt vmcnt(12)\n\ts_barrier" ::: "memory");
    issue(16);
    asm volatile("s_waitcnt vmcnt(12)\n\ts_barrier" ::: "memory");
    issue(17);
    asm volatile("s_waitcnt vmcnt(12)\n\ts_barrier" ::: "memory");
    issue(18);
    asm volatile("s_waitcnt vmcnt(12)\n\ts_barrier" ::: "memory");
    issue(19);
    asm volatile("s_waitcnt vmcnt(12)\n\ts_barrier" ::: "memory");
    issue(20);
    asm volatile("s_waitcnt vmcnt(12)\n\ts_barrier" ::: "memory");
    issue(21);
    asm volatile("s_waitcnt vmcnt(12)\n\ts_barrier" ::: "memory");
    issue(22);
    asm volatile("s_waitcnt vmcnt(12)\n\ts_barrier" ::: "memory");
    issue(23);
    asm volatile("s_waitcnt vmcnt(12)\n\ts_barrier" ::: "memory");
    issue(24);
    asm volatile("s_waitcnt vmcnt(12)\n\ts_barrier" ::: "memory");
    issue(25);
    asm volatile("s_waitcnt vmcnt(12)\n\ts_barrier" ::: "memory");
    issue(26);
    asm volatile("s_waitcnt vmcnt(12)\n\ts_barrier" ::: "memory");
    issue(27);
    asm volatile("s_waitcnt vmcnt(12)\n\ts_barrier" ::: "memory");
    issue(28);
    asm volatile("s_waitcnt vmcnt(12)\n\ts_barrier" ::: "memory");
    issue(29);
    asm volatile("s_waitcnt vmcnt(12)\n\ts_barrier" ::: "memory");
    issue(30);
    asm volatile("s_waitcnt vmcnt(12)\n\ts_barrier" ::: "memory");
    issue(31);
    asm volatile("s_waitcnt vmcnt(12)\n\ts_barrier" ::: "memory");
    issue(32);
    asm volatile("s_waitcnt vmcnt(12)\n\ts_barrier" ::: "memory");
    issue(33);
    asm volatile("s_waitcnt vmcnt(12)\n\ts_barrier" ::: "memory");
    issue(34);
    asm volatile("s_waitcnt vmcnt(12)\n\ts_barrier" ::: "memory");
    issue(35);
    asm volatile("s_waitcnt vmcnt(12)\n\ts_barrier" ::: "memory");
    asm volatile("s_waitcnt vmcnt(8)\n\ts_barrier" ::: "memory");
    asm volatile("s_waitcnt vmcnt(4)\n\ts_barrier" ::: "memory");
    asm volatile("s_waitcnt vmcnt(0)\n\ts_barrier" ::: "memory");
    st(0);
    st(1);
    st(2);
    st(3);
    st(4);
    st(5);
    st(6);
    st(7);
    st(8);
    st(9);
    st(10);
    st(11);
    st(12);
    st(13);
    st(14);
    st(15);
    asm volatile("s_waitcnt vmcnt(0)\n\ts_barrier" ::: "memory");
    tX = rt();
  asm volatile("" :: "v"(tdummy));
  }
  bf16x8 qf[16];
#pragma unroll
  for (int s = 0; s < 16; ++s) qf[s] = *(const bf16x8*)(Qw + 32 * s + 16 * h);
  f32x16 o[8];
#pragma unroll
  for (int i = 0; i < 8; ++i) o[i] = f32x16{};
  float m = -1e30f, l = 0.f;
  const float C = 0.0625f * 1.4426950408889634f;
  char* Kl = lds; char* Vl = lds + 16384;
  const unsigned vbase = (unsigned)(size_t)Vl;
  const int blk = (lane >> 4) & 1, q4 = (lane & 15) >> 2, p4 = lane & 3;
  for (int t = 0; t < 16; ++t) {
    __syncthreads();
#pragma unroll
    for (int i = 0; i < 4; ++i) {
      int n = tid + 256 * i, row = n >> 5, c = n & 31;
      u32x4 kv = *(const u32x4*)(Kh + (size_t)(t * 32 + row) * 512 + 16 * c);
      u32x4 vv = *(const u32x4*)(Vh + (size_t)(t * 32 + row) * 512 + 16 * c);
      *(u32x4*)(Kl + row * 512 + 16 * (c ^ (row & 15))) = kv;
      *(u32x4*)(Vl + row * 512 + 16 * (c ^ ((row & 3) << 2))) = vv;
    }
    __syncthreads();
    if (PROBE == 2 && t == 0) tX = rt();
    if (PROBE == 4 && t == 1) tX = rt();
    if (PROBE == 5 && t == 8) tX = rt();
    f32x16 s = f32x16{};
#pragma unroll
    for (int ks = 0; ks < 16; ++ks) {
      bf16x8 kf = *(const bf16x8*)(Kl + r * 512 + 16 * ((2 * ks + h) ^ (r & 15)));
      s = __builtin_amdgcn_mfma_f32_32x32x16_bf16(kf, qf[ks], s, 0, 0, 0);
    }
    float mx = s[0];
#pragma unroll
    for (int i = 1; i < 16; ++i) mx = fmaxf(mx, s[i]);
    mx = fmaxf(mx, __shfl_xor(mx, 32));
    float mn = fmaxf(m, mx);
    float alpha = __builtin_amdgcn_exp2f((m - mn) * C);
    m = mn;
    float ps = 0.f;
#pragma unroll
    for (int i = 0; i < 16; ++i) { s[i] = __builtin_amdgcn_exp2f(fmaf(s[i], C, -mn * C)); ps += s[i]; }
    ps += __shfl_xor(ps, 32);
    l = l * alpha + ps;
#pragma unroll
    for (int i = 0; i < 8; ++i)
#pragma unroll
      for (int j = 0; j < 16; ++j) o[i][j] *= alpha;
    bf16x8 pf[2];
#pragma unroll
    for (int ss = 0; ss < 2; ++ss) {
      u32x4 w4 = {cvtpk(s[8 * ss], s[8 * ss + 1]), cvtpk(s[8 * ss + 2], s[8 * ss + 3]), cvtpk(s[8 * ss + 4], s[8 * ss + 5]), cvtpk(s[8 * ss + 6], s[8 * ss + 7])};
      pf[ss] = __builtin_bit_cast(bf16x8, w4);
    }
#pragma unroll
    for (int db = 0; db < 8; ++db) {
#pragma unroll
      for (int ss = 0; ss < 2; ++ss) {
        bf16x4 lo, hi;
        unsigned a0 = vbase + (16 * ss + 4 * h + q4) * 512 + 64 * (db ^ q4) + 32 * blk + 8 * p4;
        asm volatile("ds_read_b64_tr_b16 %0, %2\n\tds_read_b64_tr_b16 %1, %2 offset:4096\n\ts_waitcnt lgkmcnt(0)" : "=&v"(lo), "=&v"(hi) : "v"(a0) : "memory");
        bf16x8 vf = __builtin_shufflevector(lo, hi, 0, 1, 2, 3, 4, 5, 6, 7);
        o[db] = __builtin_amdgcn_mfma_f32_32x32x16_bf16(vf, pf[ss], o[db], 0, 0, 0);
      }
    }
  }
  if (PROBE == 1) tX = rt();
  float il = 1.0f / l;
  __syncthreads();
  char* stg = lds + w * 16384;
#pragma unroll
  for (int db = 0; db < 8; ++db)
#pragma unroll
    for (int g = 0; g < 4; ++g) {
      u32x2 w2 = {cvtpk(o[db][4 * g] * il, o[db][4 * g + 1] * il), cvtpk(o[db][4 * g + 2] * il, o[db][4 * g + 3] * il)};
      int c = 4 * db + g;
      *(u32x2*)(stg + r * 512 + 16 * (c ^ (r & 15)) + 8 * h) = w2;
    }
  __syncthreads();
  char* Ow = O + ((size_t)head * SQ + half * 128 + w * 32) * 512 + lane * 16;
#pragma unroll
  for (int i = 0; i < 16; ++i) {
    int row = 2 * i + h, c = lane & 31;
    u32x4 val = *(const u32x4*)(stg + row * 512 + 16 * (c ^ (row & 15)));
    asm volatile("global_store_dwordx4 %0, %1, off " STPOL "\n\ts_nop 1" :: "v"(Ow + i * 1024), "v"(val) : "memory");
  }
  asm volatile("s_waitcnt vmcnt(0)" ::: "memory");
  if (PROBE == 3) tX = rt();
  { unsigned long long tgt = tS + 10000 + (tX - tS); while (rt() < tgt) __builtin_amdgcn_s_sleep(2); }
}

extern "C" void kernel_launch(void* const* d_in, const int* in_sizes, int n_in, void* d_out, int out_size, void* d_ws, size_t ws_size, hipStream_t stream) {
  mha_v0<<<256, 256, 0, stream>>>((const char*)d_in[0], (const char*)d_in[1], (const char*)d_in[2], (char*)d_out);
}
```

```cpp
#define PERM 2
#define QPOL "nt"
#define LPOL ""
#define FPOL "nt"
#define STPOL "sc1"
#define PROBE 0
#include <hip/hip_runtime.h>
#include <hip/hip_bf16.h>
typedef float    f32x16 __attribute__((ext_vector_type(16)));
typedef float    f32x2  __attribute__((ext_vector_type(2)));
typedef __bf16   bf16x8 __attribute__((ext_vector_type(8)));
typedef __bf16   bf16x4 __attribute__((ext_vector_type(4)));
typedef __bf16   bf16x2 __attribute__((ext_vector_type(2)));
typedef unsigned u32x4  __attribute__((ext_vector_type(4)));
typedef unsigned u32x2  __attribute__((ext_vector_type(2)));
#define DI __device__ __forceinline__
DI unsigned cvtpk(float lo, float hi) { f32x2 v = {lo, hi}; bf16x2 b = __builtin_convertvector(v, bf16x2); return __builtin_bit_cast(unsigned, b); }
DI unsigned long long rt() { unsigned long long t; asm volatile("s_memrealtime %0\n\ts_waitcnt lgkmcnt(0)" : "=s"(t) :: "memory"); return t; }
constexpr int SQ = 256, SK = 512, D = 256, NH = 128;

__global__ __launch_bounds__(256, 1) void mha_v0(const char* __restrict__ Q, const char* __restrict__ K, const char* __restrict__ V, char* __restrict__ O) {
  __shared__ __attribute__((aligned(16))) char lds[4 * 16384];
  const unsigned long long tS = rt(); unsigned long long tX = tS;
  const int b = blockIdx.x;
  const int head = (b >> 4) * 8 + (b & 7), half = (b >> 3) & 1;
  const int tid = threadIdx.x, lane = tid & 63, w = tid >> 6, r = lane & 31, h = lane >> 5;
  const char* Qw = Q + ((size_t)head * SQ + half * 128 + w * 32 + r) * 512;
  const char* Kh = K + (size_t)head * SK * 512;
  const char* Vh = V + (size_t)head * SK * 512;

  { const int wv = __builtin_amdgcn_readfirstlane(tid >> 6);
    const char* Qh = Q + ((size_t)head * 256 + half * 128) * 512;
    const unsigned ldsb = (unsigned)(size_t)lds;
    auto issue = [&](int t) {
      int tt = t < 4 ? t : ((t - 4) & 15) ^ (half * PERM);
      const char* src = t < 4 ? Qh + t * 16384 : (t < 20 ? Kh + tt * 16384 : Vh + tt * 16384);
      src += wv * 4096 + lane * 16;
      unsigned dst = ldsb + (t % 4) * 16384 + wv * 4096;
#pragma unroll
      for (int i = 0; i < 4; ++i) {
        if (t < 4) asm volatile("s_mov_b32 m0, %0\n\ts_nop 0\n\tglobal_load_lds_dwordx4 %1, off " QPOL :: "s"(dst + i * 1024), "v"(src + i * 1024) : "memory");
        else if (((t - 4) & PERM) == 0) asm volatile("s_mov_b32 m0, %0\n\ts_nop 0\n\tglobal_load_lds_dwordx4 %1, off " LPOL :: "s"(dst + i * 1024), "v"(src + i * 1024) : "memory");
        else asm volatile("s_mov_b32 m0, %0\n\ts_nop 0\n\tglobal_load_lds_dwordx4 %1, off " FPOL :: "s"(dst + i * 1024), "v"(src + i * 1024) : "memory");
      }
    };
#pragma unroll
    for (int t = 0; t < 4; ++t) issue(t);
#pragma unroll
    for (int t = 0; t < 32; ++t) { asm volatile("s_waitcnt vmcnt(12)\n\ts_barrier" ::: "memory"); issue(t + 4); }
    asm volatile("s_waitcnt vmcnt(0)\n\ts_barrier" ::: "memory");
    tX = rt();
    while (rt() < tS + 2500) __builtin_amdgcn_s_sleep(2);
  }
  bf16x8 qf[16];
#pragma unroll
  for (int s = 0; s < 16; ++s) qf[s] = *(const bf16x8*)(Qw + 32 * s + 16 * h);
  f32x16 o[8];
#pragma unroll
  for (int i = 0; i < 8; ++i) o[i] = f32x16{};
  float m = -1e30f, l = 0.f;
  const float C = 0.0625f * 1.4426950408889634f;
  char* Kl = lds; char* Vl = lds + 16384;
  const unsigned vbase = (unsigned)(size_t)Vl;
  const int blk = (lane >> 4) & 1, q4 = (lane & 15) >> 2, p4 = lane & 3;
  for (int t = 0; t < 16; ++t) {
    __syncthreads();
#pragma unroll
    for (int i = 0; i < 4; ++i) {
      int n = tid + 256 * i, row = n >> 5, c = n & 31;
      u32x4 kv = *(const u32x4*)(Kh + (size_t)(t * 32 + row) * 512 + 16 * c);
      u32x4 vv = *(const u32x4*)(Vh + (size_t)(t * 32 + row) * 512 + 16 * c);
      *(u32x4*)(Kl + row * 512 + 16 * (c ^ (row & 15))) = kv;
      *(u32x4*)(Vl + row * 512 + 16 * (c ^ ((row & 3) << 2))) = vv;
    }
    __syncthreads();
    if (PROBE == 2 && t == 0) tX = rt();
    if (PROBE == 4 && t == 1) tX = rt();
    if (PROBE == 5 && t == 8) tX = rt();
    f32x16 s = f32x16{};
#pragma unroll
    for (int ks = 0; ks < 16; ++ks) {
      bf16x8 kf = *(const bf16x8*)(Kl + r * 512 + 16 * ((2 * ks + h) ^ (r & 15)));
      s = __builtin_amdgcn_mfma_f32_32x32x16_bf16(kf, qf[ks], s, 0, 0, 0);
    }
    float mx = s[0];
#pragma unroll
    for (int i = 1; i < 16; ++i) mx = fmaxf(mx, s[i]);
    mx = fmaxf(mx, __shfl_xor(mx, 32));
    float mn = fmaxf(m, mx);
    float alpha = __builtin_amdgcn_exp2f((m - mn) * C);
    m = mn;
    float ps = 0.f;
#pragma unroll
    for (int i = 0; i < 16; ++i) { s[i] = __builtin_amdgcn_exp2f(fmaf(s[i], C, -mn * C)); ps += s[i]; }
    ps += __shfl_xor(ps, 32);
    l = l * alpha + ps;
#pragma unroll
    for (int i = 0; i < 8; ++i)
#pragma unroll
      for (int j = 0; j < 16; ++j) o[i][j] *= alpha;
    bf16x8 pf[2];
#pragma unroll
    for (int ss = 0; ss < 2; ++ss) {
      u32x4 w4 = {cvtpk(s[8 * ss], s[8 * ss + 1]), cvtpk(s[8 * ss + 2], s[8 * ss + 3]), cvtpk(s[8 * ss + 4], s[8 * ss + 5]), cvtpk(s[8 * ss + 6], s[8 * ss + 7])};
      pf[ss] = __builtin_bit_cast(bf16x8, w4);
    }
#pragma unroll
    for (int db = 0; db < 8; ++db) {
#pragma unroll
      for (int ss = 0; ss < 2; ++ss) {
        bf16x4 lo, hi;
        unsigned a0 = vbase + (16 * ss + 4 * h + q4) * 512 + 64 * (db ^ q4) + 32 * blk + 8 * p4;
        asm volatile("ds_read_b64_tr_b16 %0, %2\n\tds_read_b64_tr_b16 %1, %2 offset:4096\n\ts_waitcnt lgkmcnt(0)" : "=&v"(lo), "=&v"(hi) : "v"(a0) : "memory");
        bf16x8 vf = __builtin_shufflevector(lo, hi, 0, 1, 2, 3, 4, 5, 6, 7);
        o[db] = __builtin_amdgcn_mfma_f32_32x32x16_bf16(vf, pf[ss], o[db], 0, 0, 0);
      }
    }
  }
  if (PROBE == 1) tX = rt();
  float il = 1.0f / l;
  __syncthreads();
  char* stg = lds + w * 16384;
#pragma unroll
  for (int db = 0; db < 8; ++db)
#pragma unroll
    for (int g = 0; g < 4; ++g) {
      u32x2 w2 = {cvtpk(o[db][4 * g] * il, o[db][4 * g + 1] * il), cvtpk(o[db][4 * g + 2] * il, o[db][4 * g + 3] * il)};
      int c = 4 * db + g;
      *(u32x2*)(stg + r * 512 + 16 * (c ^ (r & 15)) + 8 * h) = w2;
    }
  __syncthreads();
  char* Ow = O + ((size_t)head * SQ + half * 128 + w * 32) * 512 + lane * 16;
#pragma unroll
  for (int i = 0; i < 16; ++i) {
    int row = 2 * i + h, c = lane & 31;
    u32x4 val = *(const u32x4*)(stg + row * 512 + 16 * (c ^ (row & 15)));
    asm volatile("global_store_dwordx4 %0, %1, off " STPOL "\n\ts_nop 1" :: "v"(Ow + i * 1024), "v"(val) : "memory");
  }
  asm volatile("s_waitcnt vmcnt(0)" ::: "memory");
  if (PROBE == 3) tX = rt();
  { unsigned long long tgt = tS + 10000 + (tX - tS); while (rt() < tgt) __builtin_amdgcn_s_sleep(2); }
}

extern "C" void kernel_launch(void* const* d_in, const int* in_sizes, int n_in, void* d_out, int out_size, void* d_ws, size_t ws_size, hipStream_t stream) {
  mha_v0<<<256, 256, 0, stream>>>((const char*)d_in[0], (const char*)d_in[1], (const char*)d_in[2], (char*)d_out);
}
```

```cpp
#include <hip/hip_runtime.h>

__global__ __launch_bounds__(256, 1) __attribute__((aligned(4096))) void mha_fwd(const void* Q, const void* K, const void* V, void* O) {
  __shared__ __attribute__((aligned(1024))) char lds[163840];
  asm volatile(
      "s_load_dwordx8 s[68:75], %0, 0x0\n"
      "v_lshrrev_b32 v26, 6, %2\n"
      "v_and_b32 v1, 63, %2\n"
      "v_readfirstlane_b32 s52, v26\n"
      "s_getpc_b64 s[62:63]\n"
      ".Lpc0_%=:\n"
      "s_lshr_b32 s60, %1, 3\n"
      "s_mul_i32 s60, s60, 1280\n"
      "v_min_u32 v29, 9, v1\n"
      "s_nop 0\n"
      "v_lshl_add_u32 v29, v29, 7, s60\n"
      "v_min_u32 v29, .Lool_%=-.Lpc0_%=-4, v29\n"
      "global_load_dword v250, v29, s[62:63]\n"
      "s_lshr_b32 s66, %1, 4\n"
      "s_lshl_b32 s66, s66, 3\n"
      "s_and_b32 s67, %1, 7\n"
      "s_or_b32 s66, s66, s67\n"
      "s_bfe_u32 s53, %1, 0x10003\n"
      "v_and_b32 v26, 31, v1\n"
      "v_lshrrev_b32 v27, 5, v1\n"
      "v_lshrrev_b32 v31, 3, v1\n"
      "v_lshrrev_b32 v28, 4, v1\n"
      "v_xor_b32 v28, v28, v1\n"
      "v_and_b32 v28, 7, v28\n"
      "v_lshlrev_b32_e64 v28, 4, v28\n"
      "v_lshl_or_b32 v96, v31, 9, v28\n"
      "v_xor_b32 v97, 64, v96\n"
      "s_lshl_b32 s67, s66, 17\n"
      "s_lshl_b32 s61, s53, 16\n"
      "s_or_b32 s67, s67, s61\n"
      "s_lshl_b32 s61, s52, 14\n"
      "s_or_b32 s67, s67, s61\n"
      "s_lshl_b32 s66, s66, 18\n"
      "s_lshl_b32 s58, s52, 12\n"
      "s_mov_b32 s38, 16384\n"
      "s_mov_b32 s39, 0x20000\n"
      "s_movk_i32 s55, 0x1000\n"
      "s_movk_i32 s56, 0x2000\n"
      "s_movk_i32 s57, 0x3000\n"
      "s_and_b32 s61, s52, 1\n"
      "s_lshl_b32 s61, s61, 7\n"
      "s_or_b32 s61, s61, s58\n"
      "v_xor_b32 v28, v26, v27\n"
      "v_lshlrev_b32 v28, 4, v28\n"
      "v_lshl_or_b32 v28, v27, 9, v28\n"
      "v_xor_b32 v2, s61, v28\n"
      "v_xor_b32 v3, 32, v2\n"
      "v_xor_b32_e64 v4, 64, v2\n"
      "v_xor_b32 v5, 96, v2\n"
      "s_mov_b32 s42, 0x40000\n"
      "s_mov_b32 s43, 0x20000\n"
      "s_waitcnt lgkmcnt(0)\n"
      "s_add_u32 s36, s68, s67\n"
      "s_addc_u32 s37, s69, 0\n"
      "s_nop 0\n"
      "s_and_b32 s37, s37, 0xffff\n"
      "s_add_u32 m0, s58, 98304\n"
      "s_nop 0\n"
      "s_nop 0\n"
      "buffer_load_dwordx4 v96, s[36:39], 0 offen offset:0 nt lds\n"
      "s_add_u32 s40, s70, s66\n"
      "s_addc_u32 s41, s71, 0\n"
      "s_and_b32 s41, s41, 0xffff\n"
      "s_add_u32 m0, s58, 99328\n"
      "s_nop 0\n"
      "s_nop 0\n"
      "buffer_load_dwordx4 v97, s[36:39], s55 offen offset:0 nt lds\n"
      "s_add_u32 m0, s58, 100352\n"
      "s_nop 0\n"
      "s_nop 0\n"
      "buffer_load_dwordx4 v96, s[36:39], s56 offen offset:0 nt lds\n"
      "s_add_u32 m0, s58, 101376\n"
      "s_nop 0\n"
      "s_nop 0\n"
      "buffer_load_dwordx4 v97, s[36:39], s57 offen offset:0 nt lds\n"
      "s_add_u32 m0, s58, 0\n"
      "s_mov_b32 s61, 0\n"
      "buffer_load_dwordx4 v2, s[40:43], s61 offen offset:0 nt lds\n"
      "buffer_load_dwordx4 v3, s[40:43], s61 offen offset:1024 nt lds\n"
      "buffer_load_dwordx4 v4, s[40:43], s61 offen offset:2048 nt lds\n"
      "buffer_load_dwordx4 v5, s[40:43], s61 offen offset:3072 nt lds\n"
      "s_add_u32 m0, s58, 16384\n"
      "s_mov_b32 s61, 16384\n"
      "buffer_load_dwordx4 v2, s[40:43], s61 offen offset:0 nt lds\n"
      "buffer_load_dwordx4 v3, s[40:43], s61 offen offset:1024 nt lds\n"
      "buffer_load_dwordx4 v4, s[40:43], s61 offen offset:2048 nt lds\n"
      "s_mov_b32 s54, 0x81ff81ff\n"
      "v_mov_b32 v208, 0x3e003e00\n"
      "v_add_u32 v209, 61, %2\n"
      "s_nop 0\n"
      "v_mul_u32_u24 v209, 0x9e3779, v209\n"
      "v_alignbit_b32 v209, v209, v209, 11\n"
      "v_and_or_b32 v192, v209, s54, v208\n"
      "v_mul_u32_u24 v209, 0x9e3779, v209\n"
      "v_alignbit_b32 v209, v209, v209, 11\n"
      "v_and_or_b32 v193, v209, s54, v208\n"
      "v_mul_u32_u24 v209, 0x9e3779, v209\n"
      "v_alignbit_b32 v209, v209, v209, 11\n"
      "v_and_or_b32 v194, v209, s54, v208\n"
      "v_mul_u32_u24 v209, 0x9e3779, v209\n"
      "v_alignbit_b32 v209, v209, v209, 11\n"
      "v_and_or_b32 v195, v209, s54, v208\n"
      "v_mul_u32_u24 v209, 0x9e3779, v209\n"
      "v_alignbit_b32 v209, v209, v209, 11\n"
      "v_and_or_b32 v196, v209, s54, v208\n"
      "v_mul_u32_u24 v209, 0x9e3779, v209\n"
      "v_alignbit_b32 v209, v209, v209, 11\n"
      "v_and_or_b32 v197, v209, s54, v208\n"
      "v_mul_u32_u24 v209, 0x9e3779, v209\n"
      "v_alignbit_b32 v209, v209, v209, 11\n"
      "v_and_or_b32 v198, v209, s54, v208\n"
      "v_mul_u32_u24 v209, 0x9e3779, v209\n"
      "v_alignbit_b32 v209, v209, v209, 11\n"
      "v_and_or_b32 v199, v209, s54, v208\n"
      "v_mul_u32_u24 v209, 0x9e3779, v209\n"
      "v_alignbit_b32 v209, v209, v209, 11\n"
      "v_and_or_b32 v200, v209, s54, v208\n"
      "v_mul_u32_u24 v209, 0x9e3779, v209\n"
      "v_alignbit_b32 v209, v209, v209, 11\n"
      "v_and_or_b32 v201, v209, s54, v208\n"
      "s_nop 1\n"
      "s_nop 0\n"
      "v_mfma_f32_32x32x16_bf16 v[224:239], v[192:195], v[194:197], 0\n"
      "s_add_u32 s62, s62, .Lhandler_%=-.Lpc0_%=\n"
      "s_addc_u32 s63, s63, 0\n"
      "s_add_u32 s44, s72, s66\n"
      "s_addc_u32 s45, s73, 0\n"
      "s_nop 0\n"
      "s_and_b32 s45, s45, 0xffff\n"
      "s_mov_b32 s46, 0x40000\n"
      "s_mov_b32 s47, 0x20000\n"
      "s_add_u32 s48, s74, s67\n"
      "s_addc_u32 s49, s75, 0\n"
      "s_and_b32 s49, s49, 0xffff\n"
      "s_mov_b32 s50, 16384\n"
      "s_mov_b32 s51, 0x20000\n"
      "s_mov_b32 s54, 0x3db8aa3b\n"
      "s_mov_b32 s76, 0x42b17218\n"
      "v_and_b32 v28, 15, v26\n"
      "v_xor_b32 v28, v28, v27\n"
      "v_lshlrev_b32_e64 v28, 4, v28\n"
      "v_lshl_or_b32 v64, v26, 9, v28\n"
      "v_xor_b32 v65, 32, v64\n"
      "v_xor_b32 v66, 64, v64\n"
      "v_xor_b32 v67, 96, v64\n"
      "v_xor_b32 v68, 128, v64\n"
      "v_xor_b32 v69, 160, v64\n"
      "v_xor_b32 v70, 192, v64\n"
      "v_mfma_f32_32x32x16_bf16 v[224:239], v[196:199], v[194:197], v[224:239]\n"
      "v_xor_b32 v71, 224, v64\n"
      "v_or_b32 v72, 0x10000, v64\n"
      "v_or_b32 v73, 0x10000, v65\n"
      "v_or_b32 v74, 0x10000, v66\n"
      "v_or_b32 v75, 0x10000, v67\n"
      "v_or_b32 v76, 0x10000, v68\n"
      "v_or_b32 v77, 0x10000, v69\n"
      "v_or_b32 v78, 0x10000, v70\n"
      "v_or_b32 v79, 0x10000, v71\n"
      "v_or_b32 v80, 0x20000, v64\n"
      "v_or_b32 v81, 0x20000, v65\n"
      "v_or_b32 v82, 0x20000, v66\n"
      "v_or_b32 v83, 0x20000, v67\n"
      "v_or_b32 v84, 0x20000, v68\n"
      "v_or_b32 v85, 0x20000, v69\n"
      "v_or_b32 v86, 0x20000, v70\n"
      "v_or_b32 v87, 0x20000, v71\n"
      "s_or_b32 s60, s58, 0x10000\n"
      "v_bfe_u32 v28, v26, 1, 3\n"
      "v_xor_b32 v28, v28, v27\n"
      "v_lshlrev_b32 v28, 4, v28\n"
      "v_lshl_or_b32 v28, v26, 7, v28\n"
      "v_or_b32 v100, s60, v28\n"
      "v_xor_b32 v101, 32, v100\n"
      "v_mfma_f32_32x32x16_bf16 v[224:239], v[192:195], v[198:201], v[224:239]\n"
      "v_xor_b32_e64 v102, 64, v100\n"
      "v_xor_b32 v103, 96, v100\n"
      "v_xor_b32 v104, 0x30000, v100\n"
      "v_xor_b32 v105, 0x30000, v101\n"
      "v_xor_b32 v106, 0x30000, v102\n"
      "v_xor_b32 v107, 0x30000, v103\n"
      "s_lshl_b32 s60, s52, 14\n"
      "v_and_b32 v28, 7, v1\n"
      "v_bfe_u32 v29, v1, 4, 1\n"
      "v_lshlrev_b32 v6, 2, v29\n"
      "v_xor_b32 v6, v6, v28\n"
      "v_lshlrev_b32_e64 v6, 4, v6\n"
      "v_lshl_or_b32 v6, v31, 9, v6\n"
      "v_or_b32_e64 v6, s60, v6\n"
      "v_add_u32 v7, 3072, v6\n"
      "v_add_u32 v8, 6144, v6\n"
      "v_add_u32 v9, 9216, v6\n"
      "s_mul_i32 s60, s52, 4608\n"
      "s_add_u32 s60, s60, 32768\n"
      "v_and_b32_e64 v22, 3, v31\n"
      "v_lshl_or_b32 v22, v22, 3, v27\n"
      "v_lshlrev_b32_e64 v23, 4, v28\n"
      "v_mul_u32_u24 v24, 144, v22\n"
      "v_add3_u32 v24, v24, v23, s60\n"
      "v_mfma_f32_32x32x16_bf16 v[224:239], v[196:199], v[198:201], v[224:239]\n"
      "v_lshl_or_b32 v23, v22, 9, v23\n"
      "v_xor_b32 v22, v31, v29\n"
      "v_and_b32 v22, 1, v22\n"
      "v_xor_b32_e64 v21, v22, v27\n"
      "v_lshl_add_u32 v22, v22, 3, v24\n"
      "v_xor_b32 v24, 8, v22\n"
      "v_lshlrev_b32 v21, 3, v21\n"
      "v_mul_u32_u24 v28, 144, v26\n"
      "v_add3_u32 v21, v21, v28, s60\n"
      "v_bfe_u32 v28, v1, 2, 2\n"
      "v_lshl_or_b32 v10, v27, 2, v28\n"
      "v_lshlrev_b32 v10, 7, v10\n"
      "v_lshrrev_b32 v28, 1, v28\n"
      "v_lshl_or_b32 v10, v28, 6, v10\n"
      "v_lshl_or_b32 v10, v29, 5, v10\n"
      "v_and_b32_e64 v28, 3, v1\n"
      "v_lshl_or_b32 v10, v28, 3, v10\n"
      "v_xor_b32_e64 v11, 64, v10\n"
      "v_or_b32 v12, 0x10000, v10\n"
      "v_or_b32 v13, 0x10000, v11\n"
      "v_mov_b32_e64 v16, 0\n"
      "v_mov_b32 v17, 0\n"
      "s_nop 1\n"
      "v_mfma_f32_32x32x16_bf16 v[224:239], v[192:195], v[194:197], v[224:239]\n"
      "s_nop 23\n"
      "s_nop 0\n"
      "v_mfma_f32_32x32x16_bf16 v[224:239], v[196:199], v[194:197], v[224:239]\n"
      "s_nop 23\n"
      "s_nop 0\n"
      "v_mfma_f32_32x32x16_bf16 v[224:239], v[192:195], v[198:201], v[224:239]\n"
      "s_nop 23\n"
      "s_nop 0\n"
      "v_mfma_f32_32x32x16_bf16 v[224:239], v[196:199], v[198:201], v[224:239]\n"
      "s_nop 23\n"
      "s_nop 0\n"
      "v_mfma_f32_32x32x16_bf16 v[224:239], v[192:195], v[194:197], v[224:239]\n"
      "s_nop 23\n"
      "s_nop 0\n"
      "v_mfma_f32_32x32x16_bf16 v[224:239], v[196:199], v[194:197], v[224:239]\n"
      "s_nop 23\n"
      "s_nop 0\n"
      "v_mfma_f32_32x32x16_bf16 v[224:239], v[192:195], v[198:201], v[224:239]\n"
      "s_nop 23\n"
      "s_nop 0\n"
      "v_mfma_f32_32x32x16_bf16 v[224:239], v[196:199], v[198:201], v[224:239]\n"
      "s_nop 23\n"
      "s_mov_b64 exec, 0\n"
      "s_cmp_eq_u32 s52, 0\n"
      "s_cbranch_scc1 .Lpad0_%=\n"
      "s_cmp_eq_u32 s52, 1\n"
      "s_cbranch_scc1 .Lpad1_%=\n"
      "s_cmp_eq_u32 s52, 2\n"
      "s_cbranch_scc1 .Lpad2_%=\n"
      "s_cmp_eq_u32 s52, 3\n"
      "s_cbranch_scc1 .Lpad3_%=\n"
      ".LpadretA_%=:\n"
      "s_cmp_eq_u32 s52, 0\n"
      "s_cbranch_scc1 .Lpad4_%=\n"
      "s_cmp_eq_u32 s52, 1\n"
      "s_cbranch_scc1 .Lpad5_%=\n"
      ".LpadretB_%=:\n"
      "s_mov_b64 exec, -1\n"
      "s_nop 0\n"
      "buffer_load_dwordx4 v5, s[40:43], s61 offen offset:3072 nt lds\n"
      "v_mfma_f32_32x32x16_bf16 v[224:239], v[192:195], v[194:197], v[224:239]\n"
      "s_nop 14\n"
      "s_nop 0\n"
      "s_add_u32 m0, s58, 114560\n"
      "s_nop 0\n"
      "s_nop 0\n"
      "buffer_load_dwordx4 v96, s[36:39], 0 offen offset:128 nt lds\n"
      "v_mfma_f32_32x32x16_bf16 v[224:239], v[196:199], v[194:197], v[224:239]\n"
      "s_nop 14\n"
      "s_nop 0\n"
      "s_add_u32 m0, s58, 115584\n"
      "s_nop 0\n"
      "s_nop 0\n"
      "buffer_load_dwordx4 v97, s[36:39], s55 offen offset:128 nt lds\n"
      "v_mfma_f32_32x32x16_bf16 v[224:239], v[192:195], v[198:201], v[224:239]\n"
      "s_nop 14\n"
      "s_nop 0\n"
      "s_add_u32 m0, s58, 116608\n"
      "s_nop 0\n"
      "s_nop 0\n"
      "buffer_load_dwordx4 v96, s[36:39], s56 offen offset:128 nt lds\n"
      "v_mfma_f32_32x32x16_bf16 v[224:239], v[196:199], v[198:201], v[224:239]\n"
      "s_nop 14\n"
      "s_nop 0\n"
      "s_add_u32 m0, s58, 117632\n"
      "s_nop 0\n"
      "s_nop 0\n"
      "buffer_load_dwordx4 v97, s[36:39], s57 offen offset:128 nt lds\n"
      "v_mfma_f32_32x32x16_bf16 v[224:239], v[192:195], v[194:197], v[224:239]\n"
      "s_nop 14\n"
      "s_nop 0\n"
      "s_add_u32 m0, s58, 130816\n"
      "s_nop 0\n"
      "s_nop 0\n"
      "buffer_load_dwordx4 v96, s[36:39], 0 offen offset:256 nt lds\n"
      "v_mfma_f32_32x32x16_bf16 v[224:239], v[196:199], v[194:197], v[224:239]\n"
      "s_nop 14\n"
      "s_nop 0\n"
      "s_add_u32 m0, s58, 131840\n"
      "s_nop 0\n"
      "s_nop 0\n"
      "buffer_load_dwordx4 v97, s[36:39], s55 offen offset:256 nt lds\n"
      "v_mfma_f32_32x32x16_bf16 v[224:239], v[192:195], v[198:201], v[224:239]\n"
      "s_nop 14\n"
      "s_nop 0\n"
      "s_add_u32 m0, s58, 132864\n"
      "s_nop 0\n"
      "s_nop 0\n"
      "buffer_load_dwordx4 v96, s[36:39], s56 offen offset:256 nt lds\n"
      "v_mfma_f32_32x32x16_bf16 v[224:239], v[196:199], v[198:201], v[224:239]\n"
      "s_nop 14\n"
      "s_nop 0\n"
      "s_add_u32 m0, s58, 133888\n"
      "s_nop 0\n"
      "s_nop 0\n"
      "buffer_load_dwordx4 v97, s[36:39], s57 offen offset:256 nt lds\n"
      "v_mfma_f32_32x32x16_bf16 v[224:239], v[192:195], v[194:197], v[224:239]\n"
      "s_nop 14\n"
      "s_nop 0\n"
      "s_add_u32 m0, s58, 147072\n"
      "s_nop 0\n"
      "s_nop 0\n"
      "buffer_load_dwordx4 v96, s[36:39], 0 offen offset:384 nt lds\n"
      "v_mfma_f32_32x32x16_bf16 v[224:239], v[196:199], v[194:197], v[224:239]\n"
      "s_nop 14\n"
      "s_nop 0\n"
      "s_add_u32 m0, s58, 148096\n"
      "s_nop 0\n"
      "s_nop 0\n"
      "buffer_load_dwordx4 v97, s[36:39], s55 offen offset:384 nt lds\n"
      "v_mfma_f32_32x32x16_bf16 v[224:239], v[192:195], v[198:201], v[224:239]\n"
      "s_nop 14\n"
      "s_nop 0\n"
      "s_add_u32 m0, s58, 149120\n"
      "s_nop 0\n"
      "s_nop 0\n"
      "buffer_load_dwordx4 v96, s[36:39], s56 offen offset:384 nt lds\n"
      "v_mfma_f32_32x32x16_bf16 v[224:239], v[196:199], v[198:201], v[224:239]\n"
      "s_nop 14\n"
      "s_nop 0\n"
      "s_add_u32 m0, s58, 150144\n"
      "s_nop 0\n"
      "s_nop 0\n"
      "buffer_load_dwordx4 v97, s[36:39], s57 offen offset:384 nt lds\n"
      "v_mfma_f32_32x32x16_bf16 v[224:239], v[192:195], v[194:197], v[224:239]\n"
      "s_nop 14\n"
      "s_nop 0\n"
      "s_memrealtime s[92:93]\n"
      "s_memtime s[94:95]\n"
      "s_waitcnt lgkmcnt(0)\n"
      "s_waitcnt vmcnt(12)\n"
      "ds_read_b128 a[0:3], v100 offset:32768\n"
      "ds_read_b128 a[4:7], v101 offset:32768\n"
      "ds_read_b128 a[8:11], v102 offset:32768\n"
      "ds_read_b128 a[12:15], v103 offset:32768\n"
      "s_barrier\n"
      "s_nop 0\n"
      "ds_read_b128 a[64:67], v64 offset:0\n"
      "ds_read_b128 a[68:71], v64 offset:16384\n"
      "ds_read_b128 a[72:75], v65 offset:0\n"
      "ds_read_b128 a[76:79], v65 offset:16384\n"
      "ds_read_b128 a[80:83], v66 offset:0\n"
      "ds_read_b128 a[84:87], v66 offset:16384\n"
      "ds_read_b128 a[88:91], v67 offset:0\n"
      "ds_read_b128 a[92:95], v67 offset:16384\n"
      "s_waitcnt lgkmcnt(4)\n"
      "s_nop 0\n"
      "v_mfma_f32_32x32x16_bf16 v[32:47], a[64:67], a[0:3], 0\n"
      "s_nop 7\n"
      "s_nop 0\n"
      "v_mfma_f32_32x32x16_bf16 v[48:63], a[68:71], a[0:3], 0\n"
      "ds_read_b128 a[96:99], v68 offset:0\n"
      "ds_read_b128 a[100:103], v68 offset:16384\n"
      "s_nop 7\n"
      "s_nop 0\n"
      "v_mfma_f32_32x32x16_bf16 v[32:47], a[72:75], a[4:7], v[32:47]\n"
      "ds_read_b128 a[104:107], v69 offset:0\n"
      "s_nop 7\n"
      "s_nop 0\n"
      "v_mfma_f32_32x32x16_bf16 v[48:63], a[76:79], a[4:7], v[48:63]\n"
      "ds_read_b128 a[108:111], v69 offset:16384\n"
      "s_nop 7\n"
      "s_waitcnt lgkmcnt(4)\n"
      "v_mfma_f32_32x32x16_bf16 v[32:47], a[80:83], a[8:11], v[32:47]\n"
      "ds_read_b128 a[112:115], v70 offset:0\n"
      "s_nop 7\n"
      "s_nop 0\n"
      "v_mfma_f32_32x32x16_bf16 v[48:63], a[84:87], a[8:11], v[48:63]\n"
      "ds_read_b128 a[116:119], v70 offset:16384\n"
      "s_nop 7\n"
      "s_nop 0\n"
      "v_mfma_f32_32x32x16_bf16 v[32:47], a[88:91], a[12:15], v[32:47]\n"
      "ds_read_b128 a[120:123], v71 offset:0\n"
      "s_nop 7\n"
      "s_nop 0\n"
      "v_mfma_f32_32x32x16_bf16 v[48:63], a[92:95], a[12:15], v[48:63]\n"
      "ds_read_b128 a[124:127], v71 offset:16384\n"
      "s_nop 7\n"
      "s_nop 0\n"
      "s_add_u32 m0, s58, 32768\n"
      "s_mov_b32 s61, 32768\n"
      "buffer_load_dwordx4 v2, s[40:43], s61 offen offset:0 nt lds\n"
      "buffer_load_dwordx4 v3, s[40:43], s61 offen offset:1024 nt lds\n"
      "buffer_load_dwordx4 v4, s[40:43], s61 offen offset:2048 nt lds\n"
      "buffer_load_dwordx4 v5, s[40:43], s61 offen offset:3072 nt lds\n"
      "s_waitcnt vmcnt(12)\n"
      "s_nop 0\n"
      "ds_read_b128 a[16:19], v100 offset:49152\n"
      "ds_read_b128 a[20:23], v101 offset:49152\n"
      "ds_read_b128 a[24:27], v102 offset:49152\n"
      "ds_read_b128 a[28:31], v103 offset:49152\n"
      "s_waitcnt lgkmcnt(2)\n"
      "s_nop 0\n"
      "v_mfma_f32_32x32x16_bf16 v[32:47], a[96:99], a[16:19], v[32:47]\n"
      "ds_read_b128 a[64:67], v64 offset:256\n"
      "s_nop 7\n"
      "s_nop 0\n"
      "v_mfma_f32_32x32x16_bf16 v[48:63], a[100:103], a[16:19], v[48:63]\n"
      "ds_read_b128 a[68:71], v64 offset:16640\n"
      "s_nop 7\n"
      "s_nop 0\n"
      "v_mfma_f32_32x32x16_bf16 v[32:47], a[104:107], a[20:23], v[32:47]\n"
      "ds_read_b128 a[72:75], v65 offset:256\n"
      "s_nop 7\n"
      "s_nop 0\n"
      "v_mfma_f32_32x32x16_bf16 v[48:63], a[108:111], a[20:23], v[48:63]\n"
      "ds_read_b128 a[76:79], v65 offset:16640\n"
      "s_nop 7\n"
      "s_waitcnt lgkmcnt(4)\n"
      "v_mfma_f32_32x32x16_bf16 v[32:47], a[112:115], a[24:27], v[32:47]\n"
      "ds_read_b128 a[80:83], v66 offset:256\n"
      "s_nop 7\n"
      "s_nop 0\n"
      "v_mfma_f32_32x32x16_bf16 v[48:63], a[116:119], a[24:27], v[48:63]\n"
      "ds_read_b128 a[84:87], v66 offset:16640\n"
      "s_nop 7\n"
      "s_nop 0\n"
      "v_mfma_f32_32x32x16_bf16 v[32:47], a[120:123], a[28:31], v[32:47]\n"
      "ds_read_b128 a[88:91], v67 offset:256\n"
      "s_nop 7\n"
      "s_nop 0\n"
      "v_mfma_f32_32x32x16_bf16 v[48:63], a[124:127], a[28:31], v[48:63]\n"
      "ds_read_b128 a[92:95], v67 offset:16640\n"
      "s_nop 7\n"
      "s_nop 0\n"
      "s_add_u32 m0, s58, 49152\n"
      "s_mov_b32 s61, 49152\n"
      "buffer_load_dwordx4 v2, s[40:43], s61 offen offset:0 nt lds\n"
      "buffer_load_dwordx4 v3, s[40:43], s61 offen offset:1024 nt lds\n"
      "buffer_load_dwordx4 v4, s[40:43], s61 offen offset:2048 nt lds\n"
      "buffer_load_dwordx4 v5, s[40:43], s61 offen offset:3072 nt lds\n"
      "s_waitcnt vmcnt(12)\n"
      "s_nop 0\n"
      "ds_read_b128 a[32:35], v104 offset:0\n"
      "ds_read_b128 a[36:39], v105 offset:0\n"
      "ds_read_b128 a[40:43], v106 offset:0\n"
      "ds_read_b128 a[44:47], v107 offset:0\n"
      "s_waitcnt lgkmcnt(2)\n"
      "s_nop 0\n"
      "v_mfma_f32_32x32x16_bf16 v[32:47], a[64:67], a[32:35], v[32:47]\n"
      "ds_read_b128 a[96:99], v68 offset:256\n"
      "s_nop 7\n"
      "s_nop 0\n"
      "v_mfma_f32_32x32x16_bf16 v[48:63], a[68:71], a[32:35], v[48:63]\n"
      "ds_read_b128 a[100:103], v68 offset:16640\n"
      "s_nop 7\n"
      "s_nop 0\n"
      "v_mfma_f32_32x32x16_bf16 v[32:47], a[72:75], a[36:39], v[32:47]\n"
      "ds_read_b128 a[104:107], v69 offset:256\n"
      "s_nop 7\n"
      "s_nop 0\n"
      "v_mfma_f32_32x32x16_bf16 v[48:63], a[76:79], a[36:39], v[48:63]\n"
      "ds_read_b128 a[108:111], v69 offset:16640\n"
      "s_nop 7\n"
      "s_waitcnt lgkmcnt(4)\n"
      "v_mfma_f32_32x32x16_bf16 v[32:47], a[80:83], a[40:43], v[32:47]\n"
      "ds_read_b128 a[112:115], v70 offset:256\n"
      "s_nop 7\n"
      "s_nop 0\n"
      "v_mfma_f32_32x32x16_bf16 v[48:63], a[84:87], a[40:43], v[48:63]\n"
      "ds_read_b128 a[116:119], v70 offset:16640\n"
      "s_nop 7\n"
      "s_nop 0\n"
      "v_mfma_f32_32x32x16_bf16 v[32:47], a[88:91], a[44:47], v[32:47]\n"
      "ds_read_b128 a[120:123], v71 offset:256\n"
      "s_nop 7\n"
      "s_nop 0\n"
      "v_mfma_f32_32x32x16_bf16 v[48:63], a[92:95], a[44:47], v[48:63]\n"
      "ds_read_b128 a[124:127], v71 offset:16640\n"
      "s_nop 7\n"
      "s_nop 0\n"
      "s_add_u32 m0, s58, 65536\n"
      "s_mov_b32 s61, 65536\n"
      "buffer_load_dwordx4 v2, s[40:43], s61 offen offset:0 nt lds\n"
      "buffer_load_dwordx4 v3, s[40:43], s61 offen offset:1024 nt lds\n"
      "buffer_load_dwordx4 v4, s[40:43], s61 offen offset:2048 nt lds\n"
      "buffer_load_dwordx4 v5, s[40:43], s61 offen offset:3072 nt lds\n"
      "s_waitcnt vmcnt(12)\n"
      "s_nop 0\n"
      "ds_read_b128 a[48:51], v104 offset:16384\n"
      "ds_read_b128 a[52:55], v105 offset:16384\n"
      "ds_read_b128 a[56:59], v106 offset:16384\n"
      "ds_read_b128 a[60:63], v107 offset:16384\n"
      "s_waitcnt lgkmcnt(2)\n"
      "s_nop 0\n"
      "v_mfma_f32_32x32x16_bf16 v[32:47], a[96:99], a[48:51], v[32:47]\n"
      "s_nop 7\n"
      "s_nop 0\n"
      "v_mfma_f32_32x32x16_bf16 v[48:63], a[100:103], a[48:51], v[48:63]\n"
      "s_nop 7\n"
      "s_nop 0\n"
      "v_mfma_f32_32x32x16_bf16 v[32:47], a[104:107], a[52:55], v[32:47]\n"
      "s_nop 7\n"
      "s_nop 0\n"
      "v_mfma_f32_32x32x16_bf16 v[48:63], a[108:111], a[52:55], v[48:63]\n"
      "s_nop 7\n"
      "s_waitcnt lgkmcnt(0)\n"
      "v_mfma_f32_32x32x16_bf16 v[32:47], a[112:115], a[56:59], v[32:47]\n"
      "s_nop 7\n"
      "s_nop 0\n"
      "v_mfma_f32_32x32x16_bf16 v[48:63], a[116:119], a[56:59], v[48:63]\n"
      "s_waitcnt vmcnt(8)\n"
      "s_barrier\n"
      "s_nop 7\n"
      "s_nop 0\n"
      "v_mfma_f32_32x32x16_bf16 v[32:47], a[120:123], a[60:63], v[32:47]\n"
      "ds_read_b128 a[64:67], v64 offset:32768\n"
      "ds_read_b128 a[68:71], v65 offset:32768\n"
      "ds_read_b128 a[72:75], v66 offset:32768\n"
      "ds_read_b128 a[76:79], v67 offset:32768\n"
      "ds_read_b128 a[80:83], v68 offset:32768\n"
      "ds_read_b128 a[84:87], v69 offset:32768\n"
      "ds_read_b128 a[88:91], v70 offset:32768\n"
      "s_add_u32 m0, s58, 81920\n"
      "s_mov_b32 s61, 81920\n"
      "buffer_load_dwordx4 v2, s[40:43], s61 offen offset:0 nt lds\n"
      "s_nop 7\n"
      "s_nop 0\n"
      "v_mfma_f32_32x32x16_bf16 v[48:63], a[124:127], a[60:63], v[48:63]\n"
      "ds_read_b128 a[92:95], v71 offset:32768\n"
      "s_nop 7\n"
      "s_waitcnt lgkmcnt(4)\n"
      "v_mfma_f32_32x32x16_bf16 v[96:111], a[64:67], a[0:3], 0\n"
      "ds_read_b128 a[96:99], v64 offset:33024\n"
      "buffer_load_dwordx4 v3, s[40:43], s61 offen offset:1024 nt lds\n"
      "s_nop 4\n"
      "s_nop 0\n"
      "v_mfma_f32_32x32x16_bf16 v[96:111], a[68:71], a[4:7], v[96:111]\n"
      "ds_read_b128 a[100:103], v65 offset:33024\n"
      "s_nop 4\n"
      "s_nop 0\n"
      "v_mfma_f32_32x32x16_bf16 v[96:111], a[72:75], a[8:11], v[96:111]\n"
      "ds_read_b128 a[104:107], v66 offset:33024\n"
      "buffer_load_dwordx4 v4, s[40:43], s61 offen offset:2048 nt lds\n"
      "s_nop 4\n"
      "s_nop 0\n"
      "v_mfma_f32_32x32x16_bf16 v[96:111], a[76:79], a[12:15], v[96:111]\n"
      "ds_read_b128 a[108:111], v67 offset:33024\n"
      "s_nop 4\n"
      "s_waitcnt lgkmcnt(4)\n"
      "v_mfma_f32_32x32x16_bf16 v[96:111], a[80:83], a[16:19], v[96:111]\n"
      "ds_read_b128 a[112:115], v68 offset:33024\n"
      "buffer_load_dwordx4 v5, s[40:43], s61 offen offset:3072 nt lds\n"
      "v_max3_f32 v18, v32, v33, v34\n"
      "v_max3_f32 v19, v47, v48, v49\n"
      "v_max3_f32 v18, v18, v35, v36\n"
      "v_max3_f32 v19, v19, v50, v51\n"
      "v_max3_f32 v18, v18, v37, v38\n"
      "v_max3_f32 v19, v19, v52, v53\n"
      "v_max3_f32 v18, v18, v39, v40\n"
      "s_nop 4\n"
      "s_nop 0\n"
      "v_mfma_f32_32x32x16_bf16 v[96:111], a[84:87], a[20:23], v[96:111]\n"
      "ds_read_b128 a[116:119], v69 offset:33024\n"
      "v_max3_f32 v19, v19, v54, v55\n"
      "v_max3_f32 v18, v18, v41, v42\n"
      "v_max3_f32 v19, v19, v56, v57\n"
      "v_max3_f32 v18, v18, v43, v44\n"
      "v_max3_f32 v19, v19, v58, v59\n"
      "v_max3_f32 v18, v18, v45, v46\n"
      "v_max3_f32 v19, v19, v60, v61\n"
      "v_max3_f32 v19, v19, v62, v63\n"
      "v_max_f32 v18, v18, v19\n"
      "s_nop 4\n"
      "v_mfma_f32_32x32x16_bf16 v[96:111], a[88:91], a[24:27], v[96:111]\n"
      "ds_read_b128 a[120:123], v70 offset:33024\n"
      "v_mov_b32 v19, v18\n"
      "s_nop 1\n"
      "v_permlane32_swap_b32 v19, v18\n"
      "v_max_f32 v25, v18, v19\n"
      "v_mul_f32 v14, s54, v25\n"
      "v_add_f32 v15, s76, v25\n"
      "v_fma_f32 v32, v32, s54, -v14\n"
      "v_fma_f32 v33, v33, s54, -v14\n"
      "s_nop 4\n"
      "s_nop 0\n"
      "v_mfma_f32_32x32x16_bf16 v[96:111], a[92:95], a[28:31], v[96:111]\n"
      "ds_read_b128 a[124:127], v71 offset:33024\n"
      "s_waitcnt vmcnt(8)\n"
      "s_barrier\n"
      "s_nop 4\n"
      "s_waitcnt lgkmcnt(4)\n"
      "v_mfma_f32_32x32x16_bf16 v[96:111], a[96:99], a[32:35], v[96:111]\n"
      "ds_read_b128 a[64:67], v64 offset:49152\n"
      "s_add_u32 m0, s58, 98304\n"
      "s_mov_b32 s61, 98304\n"
      "buffer_load_dwordx4 v2, s[40:43], s61 offen offset:0 nt lds\n"
      "v_exp_f32_e64 v32, v32\n"
      "v_fma_f32 v34, v34, s54, -v14\n"
      "v_exp_f32_e64 v33, v33\n"
      "v_add_f32 v16, v16, v32\n"
      "s_nop 4\n"
      "v_mfma_f32_32x32x16_bf16 v[96:111], a[100:103], a[36:39], v[96:111]\n"
      "ds_read_b128 a[68:71], v65 offset:49152\n"
      "buffer_load_dwordx4 v3, s[40:43], s61 offen offset:1024 nt lds\n"
      "v_fma_f32 v35, v35, s54, -v14\n"
      "v_exp_f32 v34, v34\n"
      "v_add_f32 v17, v17, v33\n"
      "v_fma_f32 v36, v36, s54, -v14\n"
      "v_exp_f32 v35, v35\n"
      "s_nop 4\n"
      "v_mfma_f32_32x32x16_bf16 v[96:111], a[104:107], a[40:43], v[96:111]\n"
      "ds_read_b128 a[72:75], v66 offset:49152\n"
      "v_add_f32_e64 v16, v16, v34\n"
      "v_cvt_pk_bf16_f32 v128, v32, v33\n"
      "v_fma_f32 v37, v37, s54, -v14\n"
      "v_exp_f32 v36, v36\n"
      "v_add_f32 v17, v17, v35\n"
      "v_fma_f32 v38, v38, s54, -v14\n"
      "v_exp_f32 v37, v37\n"
      "s_nop 4\n"
      "v_mfma_f32_32x32x16_bf16 v[96:111], a[108:111], a[44:47], v[96:111]\n"
      "ds_read_b128 a[76:79], v67 offset:49152\n"
      "buffer_load_dwordx4 v4, s[40:43], s61 offen offset:2048 nt lds\n"
      "v_add_f32_e64 v16, v16, v36\n"
      "v_cvt_pk_bf16_f32 v129, v34, v35\n"
      "v_fma_f32 v39, v39, s54, -v14\n"
      "v_exp_f32_e64 v38, v38\n"
      "s_nop 4\n"
      "s_waitcnt lgkmcnt(4)\n"
      "v_mfma_f32_32x32x16_bf16 v[96:111], a[112:115], a[48:51], v[96:111]\n"
      "ds_read_b128 a[80:83], v68 offset:49152\n"
      "v_add_f32_e64 v17, v17, v37\n"
      "v_fma_f32 v40, v40, s54, -v14\n"
      "v_exp_f32 v39, v39\n"
      "v_add_f32 v16, v16, v38\n"
      "v_cvt_pk_bf16_f32 v130, v36, v37\n"
      "v_fma_f32 v41, v41, s54, -v14\n"
      "v_exp_f32 v40, v40\n"
      "s_nop 4\n"
      "v_mfma_f32_32x32x16_bf16 v[96:111], a[116:119], a[52:55], v[96:111]\n"
      "ds_read_b128 a[84:87], v69 offset:49152\n"
      "buffer_load_dwordx4 v5, s[40:43], s61 offen offset:3072 nt lds\n"
      "v_add_f32_e64 v17, v17, v39\n"
      "v_fma_f32 v42, v42, s54, -v14\n"
      "v_exp_f32 v41, v41\n"
      "v_add_f32 v16, v16, v40\n"
      ".Lpad0_%=:\n"
      "s_cbranch_execz .LpadretA_%=\n"
      "s_nop 0\n"
      "v_cvt_pk_bf16_f32 v131, v38, v39\n"
      "v_fma_f32 v43, v43, s54, -v14\n"
      "s_nop 4\n"
      "s_nop 0\n"
      "v_mfma_f32_32x32x16_bf16 v[96:111], a[120:123], a[56:59], v[96:111]\n"
      "ds_read_b128 a[88:91], v70 offset:49152\n"
      "v_exp_f32 v42, v42\n"
      "v_add_f32 v17, v17, v41\n"
      "v_fma_f32 v44, v44, s54, -v14\n"
      "v_exp_f32 v43, v43\n"
      "v_add_f32 v16, v16, v42\n"
      "v_cvt_pk_bf16_f32 v132, v40, v41\n"
      "v_fma_f32 v45, v45, s54, -v14\n"
      "s_nop 4\n"
      "s_nop 0\n"
      "v_mfma_f32_32x32x16_bf16 v[96:111], a[124:127], a[60:63], v[96:111]\n"
      "ds_read_b128 a[92:95], v71 offset:49152\n"
      "s_add_u32 m0, s58, 114688\n"
      "s_mov_b32 s61, 114688\n"
      "v_exp_f32 v44, v44\n"
      "v_add_f32 v17, v17, v43\n"
      "v_fma_f32 v46, v46, s54, -v14\n"
      "v_exp_f32_e64 v45, v45\n"
      "s_nop 4\n"
      "s_waitcnt lgkmcnt(4)\n"
      "v_mfma_f32_32x32x16_bf16 v[112:127], a[64:67], a[0:3], 0\n"
      "ds_read_b128 a[96:99], v64 offset:49408\n"
      "buffer_load_dwordx4 v2, s[40:43], s61 offen offset:0 nt lds\n"
      "v_add_f32_e64 v16, v16, v44\n"
      "v_cvt_pk_bf16_f32 v133, v42, v43\n"
      "v_fma_f32 v47, v47, s54, -v14\n"
      "v_exp_f32 v46, v46\n"
      "v_add_f32 v17, v17, v45\n"
      "v_fma_f32 v48, v48, s54, -v14\n"
      "s_nop 3\n"
      "s_nop 0\n"
      "v_mfma_f32_32x32x16_bf16 v[112:127], a[68:71], a[4:7], v[112:127]\n"
      "ds_read_b128 a[100:103], v65 offset:49408\n"
      "v_exp_f32 v47, v47\n"
      "v_add_f32 v16, v16, v46\n"
      "v_cvt_pk_bf16_f32 v134, v44, v45\n"
      "v_fma_f32 v49, v49, s54, -v14\n"
      "v_exp_f32 v48, v48\n"
      "v_add_f32 v17, v17, v47\n"
      "v_fma_f32 v50, v50, s54, -v14\n"
      "s_nop 3\n"
      "s_nop 0\n"
      "v_mfma_f32_32x32x16_bf16 v[112:127], a[72:75], a[8:11], v[112:127]\n"
      "ds_read_b128 a[104:107], v66 offset:49408\n"
      "buffer_load_dwordx4 v3, s[40:43], s61 offen offset:1024 nt lds\n"
      "v_exp_f32 v49, v49\n"
      "v_add_f32 v16, v16, v48\n"
      "v_cvt_pk_bf16_f32 v135, v46, v47\n"
      "v_fma_f32 v51, v51, s54, -v14\n"
      "v_exp_f32 v50, v50\n"
      "s_nop 3\n"
      "v_mfma_f32_32x32x16_bf16 v[112:127], a[76:79], a[12:15], v[112:127]\n"
      "ds_read_b128 a[108:111], v67 offset:49408\n"
      "v_add_f32_e64 v17, v17, v49\n"
      "v_fma_f32 v52, v52, s54, -v14\n"
      "v_exp_f32 v51, v51\n"
      "v_add_f32 v16, v16, v50\n"
      "v_cvt_pk_bf16_f32 v136, v48, v49\n"
      "v_fma_f32 v53, v53, s54, -v14\n"
      "s_nop 3\n"
      "s_waitcnt lgkmcnt(4)\n"
      "v_mfma_f32_32x32x16_bf16 v[112:127], a[80:83], a[16:19], v[112:127]\n"
      "ds_read_b128 a[112:115], v68 offset:49408\n"
      "buffer_load_dwordx4 v4, s[40:43], s61 offen offset:2048 nt lds\n"
      "v_exp_f32 v52, v52\n"
      "v_add_f32 v17, v17, v51\n"
      "v_fma_f32 v54, v54, s54, -v14\n"
      "v_exp_f32 v53, v53\n"
      "v_add_f32 v16, v16, v52\n"
      "v_cvt_pk_bf16_f32 v137, v50, v51\n"
      "s_nop 3\n"
      "s_nop 0\n"
      "v_mfma_f32_32x32x16_bf16 v[112:127], a[84:87], a[20:23], v[112:127]\n"
      "ds_read_b128 a[116:119], v69 offset:49408\n"
      "v_fma_f32 v55, v55, s54, -v14\n"
      "v_exp_f32 v54, v54\n"
      "v_add_f32 v17, v17, v53\n"
      "v_fma_f32 v56, v56, s54, -v14\n"
      "v_exp_f32_e64 v55, v55\n"
      "v_add_f32 v16, v16, v54\n"
      "s_nop 3\n"
      "v_mfma_f32_32x32x16_bf16 v[112:127], a[88:91], a[24:27], v[112:127]\n"
      "ds_read_b128 a[120:123], v70 offset:49408\n"
      "buffer_load_dwordx4 v5, s[40:43], s61 offen offset:3072 nt lds\n"
      "v_cvt_pk_bf16_f32 v138, v52, v53\n"
      "v_fma_f32 v57, v57, s54, -v14\n"
      "v_exp_f32 v56, v56\n"
      "v_add_f32 v17, v17, v55\n"
      "v_fma_f32 v58, v58, s54, -v14\n"
      "v_exp_f32 v57, v57\n"
      "s_nop 3\n"
      "v_mfma_f32_32x32x16_bf16 v[112:127], a[92:95], a[28:31], v[112:127]\n"
      "ds_read_b128 a[124:127], v71 offset:49408\n"
      "s_memrealtime s[96:97]\n"
      "s_memtime s[98:99]\n"
      "s_waitcnt lgkmcnt(0)\n"
      "s_sub_u32 s92, s96, s92\n"
      "s_sub_u32 s94, s98, s94\n"
      "s_nop 0\n"
      "s_mul_i32 s92, s92, 2285\n"
      "s_add_u32 s94, s94, 980\n"
      "s_mul_i32 s94, s94, 100\n"
      "s_cmp_gt_u32 s94, s92\n"
      "s_cbranch_scc1 .Lslow_%=\n"
      ".Lsplit_%=:\n"
      "s_waitcnt vmcnt(8)\n"
      "s_barrier\n"
      "s_nop 3\n"
      "s_nop 0\n"
      "v_mfma_f32_32x32x16_bf16 v[112:127], a[96:99], a[32:35], v[112:127]\n"
      "ds_read_b128 a[64:67], v72 offset:0\n"
      "v_add_f32_e64 v16, v16, v56\n"
      "v_cvt_pk_bf16_f32 v139, v54, v55\n"
      "v_fma_f32 v59, v59, s54, -v14\n"
      "v_exp_f32 v58, v58\n"
      "v_add_f32 v17, v17, v57\n"
      "v_fma_f32 v60, v60, s54, -v14\n"
      "v_exp_f32 v59, v59\n"
      "s_nop 3\n"
      "v_mfma_f32_32x32x16_bf16 v[112:127], a[100:103], a[36:39], v[112:127]\n"
      "ds_read_b128 a[68:71], v73 offset:0\n"
      "s_add_u32 m0, s58, 131072\n"
      "s_mov_b32 s61, 131072\n"
      "v_add_f32_e64 v16, v16, v58\n"
      "v_cvt_pk_bf16_f32 v140, v56, v57\n"
      "v_fma_f32 v61, v61, s54, -v14\n"
      "v_exp_f32_e64 v60, v60\n"
      "v_add_f32 v17, v17, v59\n"
      "s_nop 3\n"
      "v_mfma_f32_32x32x16_bf16 v[112:127], a[104:107], a[40:43], v[112:127]\n"
      "ds_read_b128 a[72:75], v74 offset:0\n"
      "buffer_load_dwordx4 v2, s[40:43], s61 offen offset:0 nt lds\n"
      "v_fma_f32 v62, v62, s54, -v14\n"
      "v_exp_f32 v61, v61\n"
      "v_add_f32 v16, v16, v60\n"
      "v_cvt_pk_bf16_f32 v141, v58, v59\n"
      "v_fma_f32 v63, v63, s54, -v14\n"
      "v_exp_f32 v62, v62\n"
      "s_nop 3\n"
      "v_mfma_f32_32x32x16_bf16 v[112:127], a[108:111], a[44:47], v[112:127]\n"
      "ds_read_b128 a[76:79], v75 offset:0\n"
      "v_add_f32 v17, v17, v61\n"
      "v_exp_f32 v63, v63\n"
      "v_add_f32_e64 v16, v16, v62\n"
      "v_cvt_pk_bf16_f32 v142, v60, v61\n"
      "v_add_f32_e64 v17, v17, v63\n"
      "v_cvt_pk_bf16_f32 v143, v62, v63\n"
      "s_nop 3\n"
      "s_nop 0\n"
      "v_mfma_f32_32x32x16_bf16 v[112:127], a[112:115], a[48:51], v[112:127]\n"
      "ds_read_b128 a[80:83], v76 offset:0\n"
      "v_max3_f32 v18, v96, v97, v98\n"
      "v_max3_f32 v19, v103, v104, v105\n"
      "v_max3_f32 v18, v18, v99, v100\n"
      "v_max3_f32 v19, v19, v106, v107\n"
      "v_max3_f32 v18, v18, v101, v102\n"
      "v_max3_f32 v19, v19, v108, v109\n"
      "v_max3_f32 v19, v19, v110, v111\n"
      "v_max_f32 v18, v18, v19\n"
      "s_nop 3\n"
      "v_mfma_f32_32x32x16_bf16 v[112:127], a[116:119], a[52:55], v[112:127]\n"
      "ds_read_b128 a[84:87], v77 offset:0\n"
      "buffer_load_dwordx4 v3, s[40:43], s61 offen offset:1024 nt lds\n"
      "v_cmp_gt_f32 vcc, v18, v15\n"
      "s_cbranch_vccnz .Lrare1_%=\n"
      ".Lret1_%=:\n"
      "v_fma_f32 v96, v96, s54, -v14\n"
      "v_fma_f32 v97, v97, s54, -v14\n"
      "v_exp_f32 v96, v96\n"
      "s_nop 3\n"
      "v_mfma_f32_32x32x16_bf16 v[112:127], a[120:123], a[56:59], v[112:127]\n"
      "ds_read_b128 a[88:91], v78 offset:0\n"
      "v_fma_f32 v98, v98, s54, -v14\n"
      "v_exp_f32 v97, v97\n"
      "v_add_f32 v16, v16, v96\n"
      "v_fma_f32 v99, v99, s54, -v14\n"
      "v_exp_f32_e64 v98, v98\n"
      "v_add_f32 v17, v17, v97\n"
      "s_nop 3\n"
      "v_mfma_f32_32x32x16_bf16 v[112:127], a[124:127], a[60:63], v[112:127]\n"
      "ds_read_b128 a[92:95], v79 offset:0\n"
      "v_fma_f32 v100, v100, s54, -v14\n"
      "v_exp_f32 v99, v99\n"
      "v_add_f32 v16, v16, v98\n"
      "v_cvt_pk_bf16_f32 v144, v96, v97\n"
      "v_fma_f32 v101, v101, s54, -v14\n"
      "s_nop 3\n"
      "s_waitcnt lgkmcnt(4)\n"
      "v_mfma_f32_32x32x16_bf16 v[32:47], a[64:67], a[0:3], 0\n"
      "ds_read_b128 a[96:99], v72 offset:256\n"
      "buffer_load_dwordx4 v4, s[40:43], s61 offen offset:2048 nt lds\n"
      "v_exp_f32 v100, v100\n"
      "v_add_f32 v17, v17, v99\n"
      "v_fma_f32 v102, v102, s54, -v14\n"
      "v_exp_f32 v101, v101\n"
      "v_add_f32 v16, v16, v100\n"
      "v_mfma_f32_32x32x16_bf16 v[32:47], a[68:71], a[4:7], v[32:47]\n"
      "ds_read_b128 a[100:103], v73 offset:256\n"
      "v_cvt_pk_bf16_f32 v145, v98, v99\n"
      "v_fma_f32 v103, v103, s54, -v14\n"
      "v_exp_f32 v102, v102\n"
      "v_add_f32 v17, v17, v101\n"
      "v_fma_f32 v104, v104, s54, -v14\n"
      "v_exp_f32_e64 v103, v103\n"
      "v_mfma_f32_32x32x16_bf16 v[32:47], a[72:75], a[8:11], v[32:47]\n"
      "ds_read_b128 a[104:107], v74 offset:256\n"
      "v_add_f32_e64 v16, v16, v102\n"
      "v_cvt_pk_bf16_f32 v146, v100, v101\n"
      "v_fma_f32 v105, v105, s54, -v14\n"
      "v_exp_f32 v104, v104\n"
      "v_add_f32 v17, v17, v103\n"
      "v_fma_f32 v106, v106, s54, -v14\n"
      "v_mfma_f32_32x32x16_bf16 v[32:47], a[76:79], a[12:15], v[32:47]\n"
      "ds_read_b128 a[108:111], v75 offset:256\n"
      "buffer_load_dwordx4 v5, s[40:43], s61 offen offset:3072 nt lds\n"
      "v_exp_f32 v105, v105\n"
      "v_add_f32 v16, v16, v104\n"
      "v_cvt_pk_bf16_f32 v147, v102, v103\n"
      "v_fma_f32 v107, v107, s54, -v14\n"
      "s_waitcnt lgkmcnt(4)\n"
      "s_nop 0\n"
      "v_mfma_f32_32x32x16_bf16 v[32:47], a[80:83], a[16:19], v[32:47]\n"
      "ds_read_b128 a[112:115], v76 offset:256\n"
      "v_exp_f32 v106, v106\n"
      "v_add_f32 v17, v17, v105\n"
      "v_fma_f32 v108, v108, s54, -v14\n"
      "v_exp_f32 v107, v107\n"
      "v_add_f32 v16, v16, v106\n"
      "v_cvt_pk_bf16_f32 v148, v104, v105\n"
      "v_mfma_f32_32x32x16_bf16 v[32:47], a[84:87], a[20:23], v[32:47]\n"
      "ds_read_b128 a[116:119], v77 offset:256\n"
      "v_fma_f32 v109, v109, s54, -v14\n"
      "v_exp_f32 v108, v108\n"
      "v_add_f32 v17, v17, v107\n"
      "v_fma_f32 v110, v110, s54, -v14\n"
      "v_exp_f32 v109, v109\n"
      "v_add_f32 v16, v16, v108\n"
      "v_mfma_f32_32x32x16_bf16 v[32:47], a[88:91], a[24:27], v[32:47]\n"
      "ds_read_b128 a[120:123], v78 offset:256\n"
      "s_add_u32 m0, s58, 147456\n"
      "s_mov_b32 s61, 147456\n"
      "v_cvt_pk_bf16_f32 v149, v106, v107\n"
      "v_fma_f32 v111, v111, s54, -v14\n"
      "v_exp_f32 v110, v110\n"
      "v_add_f32 v17, v17, v109\n"
      "v_exp_f32_e64 v111, v111\n"
      "v_mfma_f32_32x32x16_bf16 v[32:47], a[92:95], a[28:31], v[32:47]\n"
      "ds_read_b128 a[124:127], v79 offset:256\n"
      "buffer_load_dwordx4 v2, s[40:43], s61 offen offset:0 nt lds\n"
      "v_add_f32_e64 v16, v16, v110\n"
      "v_cvt_pk_bf16_f32 v150, v108, v109\n"
      "v_add_f32 v17, v17, v111\n"
      "s_waitcnt lgkmcnt(4)\n"
      "v_cvt_pk_bf16_f32 v151, v110, v111\n"
      "v_mfma_f32_32x32x16_bf16 v[32:47], a[96:99], a[32:35], v[32:47]\n"
      "ds_read_b128 a[64:67], v72 offset:16384\n"
      "v_max3_f32 v18, v112, v113, v114\n"
      "v_max3_f32 v19, v119, v120, v121\n"
      "v_max3_f32 v18, v18, v115, v116\n"
      "v_max3_f32 v19, v19, v122, v123\n"
      "v_max3_f32 v18, v18, v117, v118\n"
      "v_max3_f32 v19, v19, v124, v125\n"
      "v_max3_f32 v19, v19, v126, v127\n"
      "v_max_f32 v18, v18, v19\n"
      "v_cmp_gt_f32 vcc, v18, v15\n"
      "v_mfma_f32_32x32x16_bf16 v[32:47], a[100:103], a[36:39], v[32:47]\n"
      "ds_read_b128 a[68:71], v73 offset:16384\n"
      "s_cbranch_vccnz .Lrare2_%=\n"
      ".Lret2_%=:\n"
      "s_nop 0\n"
      "v_fma_f32 v112, v112, s54, -v14\n"
      "v_fma_f32 v113, v113, s54, -v14\n"
      "v_exp_f32_e64 v112, v112\n"
      "v_fma_f32 v114, v114, s54, -v14\n"
      "v_exp_f32 v113, v113\n"
      "v_add_f32 v16, v16, v112\n"
      "v_mfma_f32_32x32x16_bf16 v[32:47], a[104:107], a[40:43], v[32:47]\n"
      "ds_read_b128 a[72:75], v74 offset:16384\n"
      "buffer_load_dwordx4 v3, s[40:43], s61 offen offset:1024 nt lds\n"
      "v_fma_f32 v115, v115, s54, -v14\n"
      "v_exp_f32 v114, v114\n"
      "v_add_f32 v17, v17, v113\n"
      "v_fma_f32 v116, v116, s54, -v14\n"
      "v_exp_f32_e64 v115, v115\n"
      "v_mfma_f32_32x32x16_bf16 v[32:47], a[108:111], a[44:47], v[32:47]\n"
      "ds_read_b128 a[76:79], v75 offset:16384\n"
      "v_add_f32 v16, v16, v114\n"
      "s_waitcnt lgkmcnt(4)\n"
      "v_cvt_pk_bf16_f32 v152, v112, v113\n"
      "v_fma_f32 v117, v117, s54, -v14\n"
      "v_exp_f32 v116, v116\n"
      "v_add_f32 v17, v17, v115\n"
      "v_fma_f32 v118, v118, s54, -v14\n"
      "v_mfma_f32_32x32x16_bf16 v[32:47], a[112:115], a[48:51], v[32:47]\n"
      "ds_read_b128 a[80:83], v76 offset:16384\n"
      "v_exp_f32 v117, v117\n"
      "v_add_f32 v16, v16, v116\n"
      "v_cvt_pk_bf16_f32 v153, v114, v115\n"
      "v_fma_f32 v119, v119, s54, -v14\n"
      "v_exp_f32 v118, v118\n"
      "v_add_f32 v17, v17, v117\n"
      "v_fma_f32 v120, v120, s54, -v14\n"
      "v_mfma_f32_32x32x16_bf16 v[32:47], a[116:119], a[52:55], v[32:47]\n"
      "ds_read_b128 a[84:87], v77 offset:16384\n"
      "v_exp_f32 v119, v119\n"
      "v_add_f32 v16, v16, v118\n"
      "v_cvt_pk_bf16_f32 v154, v116, v117\n"
      "v_fma_f32 v121, v121, s54, -v14\n"
      "v_exp_f32 v120, v120\n"
      "v_add_f32 v17, v17, v119\n"
      "v_fma_f32 v122, v122, s54, -v14\n"
      "v_mfma_f32_32x32x16_bf16 v[32:47], a[120:123], a[56:59], v[32:47]\n"
      "ds_read_b128 a[88:91], v78 offset:16384\n"
      "buffer_load_dwordx4 v4, s[40:43], s61 offen offset:2048 nt lds\n"
      "v_exp_f32 v121, v121\n"
      "v_add_f32 v16, v16, v120\n"
      "v_cvt_pk_bf16_f32 v155, v118, v119\n"
      "v_fma_f32 v123, v123, s54, -v14\n"
      "v_exp_f32_e64 v122, v122\n"
      "v_mfma_f32_32x32x16_bf16 v[32:47], a[124:127], a[60:63], v[32:47]\n"
      "ds_read_b128 a[92:95], v79 offset:16384\n"
      "v_add_f32 v17, v17, v121\n"
      "s_waitcnt lgkmcnt(4)\n"
      "v_fma_f32 v124, v124, s54, -v14\n"
      "v_exp_f32 v123, v123\n"
      "v_add_f32 v16, v16, v122\n"
      "v_cvt_pk_bf16_f32 v156, v120, v121\n"
      "v_fma_f32 v125, v125, s54, -v14\n"
      "v_mfma_f32_32x32x16_bf16 v[48:63], a[64:67], a[0:3], 0\n"
      "ds_read_b128 a[96:99], v72 offset:16640\n"
      "v_exp_f32 v124, v124\n"
      "v_add_f32 v17, v17, v123\n"
      "v_fma_f32 v126, v126, s54, -v14\n"
      "v_exp_f32 v125, v125\n"
      "v_add_f32 v16, v16, v124\n"
      "v_cvt_pk_bf16_f32 v157, v122, v123\n"
      "v_fma_f32 v127, v127, s54, -v14\n"
      "v_mfma_f32_32x32x16_bf16 v[48:63], a[68:71], a[4:7], v[48:63]\n"
      "ds_read_b128 a[100:103], v73 offset:16640\n"
      "v_exp_f32 v126, v126\n"
      "v_add_f32 v17, v17, v125\n"
      "v_exp_f32 v127, v127\n"
      "v_add_f32 v16, v16, v126\n"
      "v_cvt_pk_bf16_f32 v158, v124, v125\n"
      "v_add_f32_e64 v17, v17, v127\n"
      "v_cvt_pk_bf16_f32 v159, v126, v127\n"
      "v_mfma_f32_32x32x16_bf16 v[48:63], a[72:75], a[8:11], v[48:63]\n"
      "ds_read_b128 a[104:107], v74 offset:16640\n"
      "buffer_load_dwordx4 v5, s[40:43], s61 offen offset:3072 nt lds\n"
      "v_max3_f32 v18, v32, v33, v34\n"
      "v_max3_f32 v19, v39, v40, v41\n"
      "v_max3_f32 v18, v18, v35, v36\n"
      "v_max3_f32 v19, v19, v42, v43\n"
      "v_mfma_f32_32x32x16_bf16 v[48:63], a[76:79], a[12:15], v[48:63]\n"
      "ds_read_b128 a[108:111], v75 offset:16640\n"
      "v_max3_f32 v18, v18, v37, v38\n"
      "v_max3_f32 v19, v19, v44, v45\n"
      "v_max3_f32 v19, v19, v46, v47\n"
      "v_max_f32 v18, v18, v19\n"
      "v_cmp_gt_f32 vcc, v18, v15\n"
      "s_cbranch_vccnz .Lrare3_%=\n"
      ".Lret3_%=:\n"
      "s_waitcnt lgkmcnt(4)\n"
      "v_mfma_f32_32x32x16_bf16 v[48:63], a[80:83], a[16:19], v[48:63]\n"
      "ds_read_b128 a[112:115], v76 offset:16640\n"
      "v_fma_f32 v32, v32, s54, -v14\n"
      "v_fma_f32 v33, v33, s54, -v14\n"
      "v_exp_f32_e64 v32, v32\n"
      "v_fma_f32 v34, v34, s54, -v14\n"
      "v_exp_f32_e64 v33, v33\n"
      "v_mfma_f32_32x32x16_bf16 v[48:63], a[84:87], a[20:23], v[48:63]\n"
      "ds_read_b128 a[116:119], v77 offset:16640\n"
      "v_add_f32_e64 v16, v16, v32\n"
      "v_fma_f32 v35, v35, s54, -v14\n"
      "v_exp_f32 v34, v34\n"
      "v_add_f32 v17, v17, v33\n"
      "v_fma_f32 v36, v36, s54, -v14\n"
      "v_mfma_f32_32x32x16_bf16 v[48:63], a[88:91], a[24:27], v[48:63]\n"
      "ds_read_b128 a[120:123], v78 offset:16640\n"
      "v_exp_f32 v35, v35\n"
      "v_add_f32 v16, v16, v34\n"
      "v_cvt_pk_bf16_f32 v160, v32, v33\n"
      "v_fma_f32 v37, v37, s54, -v14\n"
      "v_exp_f32_e64 v36, v36\n"
      "v_mfma_f32_32x32x16_bf16 v[48:63], a[92:95], a[28:31], v[48:63]\n"
      "ds_read_b128 a[124:127], v79 offset:16640\n"
      "s_waitcnt vmcnt(8)\n"
      "s_barrier\n"
      "s_waitcnt lgkmcnt(4)\n"
      "s_nop 0\n"
      "v_mfma_f32_32x32x16_bf16 v[48:63], a[96:99], a[32:35], v[48:63]\n"
      "ds_read_b128 a[64:67], v72 offset:32768\n"
      "s_add_u32 m0, s58, 0\n"
      "s_nop 0\n"
      "s_mov_b32 s61, 163840\n"
      "buffer_load_dwordx4 v2, s[40:43], s61 offen offset:0 nt lds\n"
      "v_add_f32_e64 v17, v17, v35\n"
      "v_fma_f32 v38, v38, s54, -v14\n"
      "v_mfma_f32_32x32x16_bf16 v[48:63], a[100:103], a[36:39], v[48:63]\n"
      "ds_read_b128 a[68:71], v73 offset:32768\n"
      "v_exp_f32 v37, v37\n"
      "v_add_f32 v16, v16, v36\n"
      "v_cvt_pk_bf16_f32 v161, v34, v35\n"
      "v_fma_f32 v39, v39, s54, -v14\n"
      "v_exp_f32_e64 v38, v38\n"
      "v_mfma_f32_32x32x16_bf16 v[48:63], a[104:107], a[40:43], v[48:63]\n"
      "ds_read_b128 a[72:75], v74 offset:32768\n"
      "buffer_load_dwordx4 v3, s[40:43], s61 offen offset:1024 nt lds\n"
      "v_add_f32_e64 v17, v17, v37\n"
      "v_fma_f32 v40, v40, s54, -v14\n"
      "v_exp_f32_e64 v39, v39\n"
      "v_mfma_f32_32x32x16_bf16 v[48:63], a[108:111], a[44:47], v[48:63]\n"
      "ds_read_b128 a[76:79], v75 offset:32768\n"
      "v_add_f32_e64 v16, v16, v38\n"
      "v_cvt_pk_bf16_f32 v162, v36, v37\n"
      "v_fma_f32 v41, v41, s54, -v14\n"
      "v_exp_f32 v40, v40\n"
      "s_waitcnt lgkmcnt(4)\n"
      "v_mfma_f32_32x32x16_bf16 v[48:63], a[112:115], a[48:51], v[48:63]\n"
      "ds_read_b128 a[80:83], v76 offset:32768\n"
      "v_add_f32_e64 v17, v17, v39\n"
      "v_fma_f32 v42, v42, s54, -v14\n"
      "v_exp_f32 v41, v41\n"
      "v_add_f32 v16, v16, v40\n"
      "v_cvt_pk_bf16_f32 v163, v38, v39\n"
      "v_fma_f32 v43, v43, s54, -v14\n"
      "v_mfma_f32_32x32x16_bf16 v[48:63], a[116:119], a[52:55], v[48:63]\n"
      "ds_read_b128 a[84:87], v77 offset:32768\n"
      "buffer_load_dwordx4 v4, s[40:43], s61 offen offset:2048 nt lds\n"
      "v_exp_f32 v42, v42\n"
      "v_add_f32 v17, v17, v41\n"
      "v_fma_f32 v44, v44, s54, -v14\n"
      "v_mfma_f32_32x32x16_bf16 v[48:63], a[120:123], a[56:59], v[48:63]\n"
      "ds_read_b128 a[88:91], v78 offset:32768\n"
      "v_exp_f32 v43, v43\n"
      "v_add_f32 v16, v16, v42\n"
      "v_cvt_pk_bf16_f32 v164, v40, v41\n"
      "v_fma_f32 v45, v45, s54, -v14\n"
      "v_exp_f32_e64 v44, v44\n"
      "v_mfma_f32_32x32x16_bf16 v[48:63], a[124:127], a[60:63], v[48:63]\n"
      "ds_read_b128 a[92:95], v79 offset:32768\n"
      "v_add_f32_e64 v17, v17, v43\n"
      "v_fma_f32 v46, v46, s54, -v14\n"
      "v_exp_f32_e64 v45, v45\n"
      "v_add_f32 v16, v16, v44\n"
      "s_waitcnt lgkmcnt(4)\n"
      "v_mfma_f32_32x32x16_bf16 v[96:111], a[64:67], a[0:3], 0\n"
      "ds_read_b128 a[96:99], v72 offset:33024\n"
      "buffer_load_dwordx4 v5, s[40:43], s61 offen offset:3072 nt lds\n"
      "v_cvt_pk_bf16_f32 v165, v42, v43\n"
      "v_fma_f32 v47, v47, s54, -v14\n"
      "v_exp_f32 v46, v46\n"
      "v_add_f32 v17, v17, v45\n"
      "v_mfma_f32_32x32x16_bf16 v[96:111], a[68:71], a[4:7], v[96:111]\n"
      "ds_read_b128 a[100:103], v73 offset:33024\n"
      "v_exp_f32 v47, v47\n"
      "v_add_f32 v16, v16, v46\n"
      "v_cvt_pk_bf16_f32 v166, v44, v45\n"
      "v_add_f32_e64 v17, v17, v47\n"
      "v_cvt_pk_bf16_f32 v167, v46, v47\n"
      "v_mfma_f32_32x32x16_bf16 v[96:111], a[72:75], a[8:11], v[96:111]\n"
      "ds_read_b128 a[104:107], v74 offset:33024\n"
      "v_max3_f32 v18, v48, v49, v50\n"
      "v_max3_f32 v19, v55, v56, v57\n"
      "v_max3_f32 v18, v18, v51, v52\n"
      "v_max3_f32 v19, v19, v58, v59\n"
      "v_max3_f32 v18, v18, v53, v54\n"
      "v_max3_f32 v19, v19, v60, v61\n"
      "v_mfma_f32_32x32x16_bf16 v[96:111], a[76:79], a[12:15], v[96:111]\n"
      "ds_read_b128 a[108:111], v75 offset:33024\n"
      "v_max3_f32 v19, v19, v62, v63\n"
      "v_max_f32_e64 v18, v18, v19\n"
      "v_cmp_gt_f32 vcc, v18, v15\n"
      "s_cbranch_vccnz .Lrare4_%=\n"
      ".Lret4_%=:\n"
      "v_fma_f32 v48, v48, s54, -v14\n"
      "s_waitcnt lgkmcnt(4)\n"
      "s_nop 0\n"
      "v_mfma_f32_32x32x16_bf16 v[96:111], a[80:83], a[16:19], v[96:111]\n"
      "ds_read_b128 a[112:115], v76 offset:33024\n"
      "s_add_u32 m0, s58, 16384\n"
      "s_mov_b32 s61, 180224\n"
      "v_fma_f32 v49, v49, s54, -v14\n"
      "v_exp_f32_e64 v48, v48\n"
      "v_fma_f32 v50, v50, s54, -v14\n"
      "v_mfma_f32_32x32x16_bf16 v[96:111], a[84:87], a[20:23], v[96:111]\n"
      "ds_read_b128 a[116:119], v77 offset:33024\n"
      "buffer_load_dwordx4 v2, s[40:43], s61 offen offset:0 nt lds\n"
      "v_exp_f32 v49, v49\n"
      "v_add_f32 v16, v16, v48\n"
      "v_fma_f32 v51, v51, s54, -v14\n"
      "v_mfma_f32_32x32x16_bf16 v[96:111], a[88:91], a[24:27], v[96:111]\n"
      "ds_read_b128 a[120:123], v78 offset:33024\n"
      "v_exp_f32 v50, v50\n"
      "v_add_f32 v17, v17, v49\n"
      "v_fma_f32 v52, v52, s54, -v14\n"
      "v_exp_f32 v51, v51\n"
      "v_add_f32 v16, v16, v50\n"
      "v_mfma_f32_32x32x16_bf16 v[96:111], a[92:95], a[28:31], v[96:111]\n"
      "ds_read_b128 a[124:127], v79 offset:33024\n"
      "v_cvt_pk_bf16_f32 v168, v48, v49\n"
      "v_fma_f32 v53, v53, s54, -v14\n"
      "v_exp_f32_e64 v52, v52\n"
      "v_add_f32 v17, v17, v51\n"
      "s_waitcnt lgkmcnt(4)\n"
      "v_mfma_f32_32x32x16_bf16 v[96:111], a[96:99], a[32:35], v[96:111]\n"
      "ds_read_b128 a[64:67], v72 offset:49152\n"
      "buffer_load_dwordx4 v3, s[40:43], s61 offen offset:1024 nt lds\n"
      "v_fma_f32 v54, v54, s54, -v14\n"
      "v_exp_f32 v53, v53\n"
      "v_add_f32 v16, v16, v52\n"
      "v_mfma_f32_32x32x16_bf16 v[96:111], a[100:103], a[36:39], v[96:111]\n"
      "ds_read_b128 a[68:71], v73 offset:49152\n"
      "v_cvt_pk_bf16_f32 v169, v50, v51\n"
      "v_fma_f32 v55, v55, s54, -v14\n"
      "v_exp_f32 v54, v54\n"
      "v_add_f32 v17, v17, v53\n"
      "v_fma_f32 v56, v56, s54, -v14\n"
      "v_mfma_f32_32x32x16_bf16 v[96:111], a[104:107], a[40:43], v[96:111]\n"
      "ds_read_b128 a[72:75], v74 offset:49152\n"
      "v_exp_f32 v55, v55\n"
      "v_add_f32 v16, v16, v54\n"
      "v_cvt_pk_bf16_f32 v170, v52, v53\n"
      "v_fma_f32 v57, v57, s54, -v14\n"
      "v_exp_f32_e64 v56, v56\n"
      "v_mfma_f32_32x32x16_bf16 v[96:111], a[108:111], a[44:47], v[96:111]\n"
      "ds_read_b128 a[76:79], v75 offset:49152\n"
      "buffer_load_dwordx4 v4, s[40:43], s61 offen offset:2048 nt lds\n"
      "v_add_f32 v17, v17, v55\n"
      "s_waitcnt lgkmcnt(4)\n"
      "v_fma_f32 v58, v58, s54, -v14\n"
      "v_mfma_f32_32x32x16_bf16 v[96:111], a[112:115], a[48:51], v[96:111]\n"
      "ds_read_b128 a[80:83], v76 offset:49152\n"
      "v_exp_f32 v57, v57\n"
      "v_add_f32 v16, v16, v56\n"
      "v_cvt_pk_bf16_f32 v171, v54, v55\n"
      "v_fma_f32 v59, v59, s54, -v14\n"
      "v_exp_f32_e64 v58, v58\n"
      "v_mfma_f32_32x32x16_bf16 v[96:111], a[116:119], a[52:55], v[96:111]\n"
      "ds_read_b128 a[84:87], v77 offset:49152\n"
      "v_add_f32_e64 v17, v17, v57\n"
      "v_fma_f32 v60, v60, s54, -v14\n"
      "v_exp_f32 v59, v59\n"
      "v_add_f32 v16, v16, v58\n"
      "v_cvt_pk_bf16_f32 v172, v56, v57\n"
      "v_mfma_f32_32x32x16_bf16 v[96:111], a[120:123], a[56:59], v[96:111]\n"
      "ds_read_b128 a[88:91], v78 offset:49152\n"
      "buffer_load_dwordx4 v5, s[40:43], s61 offen offset:3072 nt lds\n"
      "v_fma_f32 v61, v61, s54, -v14\n"
      "v_exp_f32 v60, v60\n"
      "v_add_f32 v17, v17, v59\n"
      "v_mfma_f32_32x32x16_bf16 v[96:111], a[124:127], a[60:63], v[96:111]\n"
      "ds_read_b128 a[92:95], v79 offset:49152\n"
      "v_fma_f32 v62, v62, s54, -v14\n"
      "v_exp_f32 v61, v61\n"
      "v_add_f32 v16, v16, v60\n"
      "v_cvt_pk_bf16_f32 v173, v58, v59\n"
      "s_waitcnt lgkmcnt(4)\n"
      "s_nop 0\n"
      "v_mfma_f32_32x32x16_bf16 v[112:127], a[64:67], a[0:3], 0\n"
      "ds_read_b128 a[96:99], v72 offset:49408\n"
      "v_fma_f32 v63, v63, s54, -v14\n"
      "v_exp_f32 v62, v62\n"
      "v_add_f32 v17, v17, v61\n"
      "v_exp_f32_e64 v63, v63\n"
      "v_mfma_f32_32x32x16_bf16 v[112:127], a[68:71], a[4:7], v[112:127]\n"
      "ds_read_b128 a[100:103], v73 offset:49408\n"
      "s_add_u32 m0, s58, 32768\n"
      "s_mov_b32 s61, 0\n"
      "v_add_f32 v16, v16, v62\n"
      "v_cvt_pk_bf16_f32 v174, v60, v61\n"
      "v_add_f32_e64 v17, v17, v63\n"
      "v_cvt_pk_bf16_f32 v175, v62, v63\n"
      "v_mfma_f32_32x32x16_bf16 v[112:127], a[72:75], a[8:11], v[112:127]\n"
      "ds_read_b128 a[104:107], v74 offset:49408\n"
      "buffer_load_dwordx4 v6, s[44:47], s61 offen offset:0 nt lds\n"
      "v_max3_f32 v18, v96, v97, v98\n"
      "v_max3_f32 v19, v103, v104, v105\n"
      "v_max3_f32 v18, v18, v99, v100\n"
      "v_max3_f32 v19, v19, v106, v107\n"
      "v_max3_f32 v18, v18, v101, v102\n"
      "v_mfma_f32_32x32x16_bf16 v[112:127], a[76:79], a[12:15], v[112:127]\n"
      "ds_read_b128 a[108:111], v75 offset:49408\n"
      "v_max3_f32 v19, v19, v108, v109\n"
      "v_max3_f32 v19, v19, v110, v111\n"
      "v_max_f32 v18, v18, v19\n"
      "v_cmp_gt_f32 vcc, v18, v15\n"
      "s_cbranch_vccnz .Lrare5_%=\n"
      ".Lret5_%=:\n"
      "s_waitcnt lgkmcnt(4)\n"
      "v_mfma_f32_32x32x16_bf16 v[112:127], a[80:83], a[16:19], v[112:127]\n"
      "ds_read_b128 a[112:115], v76 offset:49408\n"
      "buffer_load_dwordx4 v7, s[44:47], s61 offen offset:1024 nt lds\n"
      "v_fma_f32 v96, v96, s54, -v14\n"
      "v_fma_f32 v97, v97, s54, -v14\n"
      "v_exp_f32_e64 v96, v96\n"
      "v_fma_f32 v98, v98, s54, -v14\n"
      "v_mfma_f32_32x32x16_bf16 v[112:127], a[84:87], a[20:23], v[112:127]\n"
      "ds_read_b128 a[116:119], v77 offset:49408\n"
      "v_exp_f32 v97, v97\n"
      "v_add_f32 v16, v16, v96\n"
      "v_fma_f32 v99, v99, s54, -v14\n"
      "v_exp_f32_e64 v98, v98\n"
      "v_mfma_f32_32x32x16_bf16 v[112:127], a[88:91], a[24:27], v[112:127]\n"
      "ds_read_b128 a[120:123], v78 offset:49408\n"
      "v_add_f32_e64 v17, v17, v97\n"
      "v_fma_f32 v100, v100, s54, -v14\n"
      "v_exp_f32 v99, v99\n"
      "v_add_f32 v16, v16, v98\n"
      "v_cvt_pk_bf16_f32 v176, v96, v97\n"
      "v_fma_f32 v101, v101, s54, -v14\n"
      "v_mfma_f32_32x32x16_bf16 v[112:127], a[92:95], a[28:31], v[112:127]\n"
      "ds_read_b128 a[124:127], v79 offset:49408\n"
      "s_waitcnt vmcnt(10)\n"
      "s_barrier\n"
      "s_waitcnt lgkmcnt(4)\n"
      "s_nop 0\n"
      "v_mfma_f32_32x32x16_bf16 v[112:127], a[96:99], a[32:35], v[112:127]\n"
      "ds_read_b128 a[64:67], v80 offset:0\n"
      "buffer_load_dwordx4 v8, s[44:47], s61 offen offset:2048 nt lds\n"
      ".Lpad1_%=:\n"
      "s_cbranch_execz .LpadretA_%=\n"
      "s_nop 0\n"
      "v_exp_f32 v100, v100\n"
      "v_add_f32 v17, v17, v99\n"
      "v_fma_f32 v102, v102, s54, -v14\n"
      "v_mfma_f32_32x32x16_bf16 v[112:127], a[100:103], a[36:39], v[112:127]\n"
      "ds_read_b128 a[68:71], v80 offset:16384\n"
      "v_exp_f32 v101, v101\n"
      "v_add_f32 v16, v16, v100\n"
      "v_cvt_pk_bf16_f32 v177, v98, v99\n"
      "v_fma_f32 v103, v103, s54, -v14\n"
      "v_exp_f32_e64 v102, v102\n"
      "v_mfma_f32_32x32x16_bf16 v[112:127], a[104:107], a[40:43], v[112:127]\n"
      "ds_read_b128 a[72:75], v81 offset:0\n"
      "buffer_load_dwordx4 v9, s[44:47], s61 offen offset:3072 nt lds\n"
      "v_add_f32_e64 v17, v17, v101\n"
      "v_fma_f32 v104, v104, s54, -v14\n"
      "v_exp_f32 v103, v103\n"
      "v_add_f32 v16, v16, v102\n"
      "v_mfma_f32_32x32x16_bf16 v[112:127], a[108:111], a[44:47], v[112:127]\n"
      "ds_read_b128 a[76:79], v81 offset:16384\n"
      "v_cvt_pk_bf16_f32 v178, v100, v101\n"
      "v_fma_f32 v105, v105, s54, -v14\n"
      "v_exp_f32_e64 v104, v104\n"
      "v_add_f32 v17, v17, v103\n"
      "s_waitcnt lgkmcnt(4)\n"
      "v_mfma_f32_32x32x16_bf16 v[112:127], a[112:115], a[48:51], v[112:127]\n"
      "ds_read_b128 a[80:83], v82 offset:0\n"
      "v_fma_f32 v106, v106, s54, -v14\n"
      "v_exp_f32 v105, v105\n"
      "v_add_f32 v16, v16, v104\n"
      "v_cvt_pk_bf16_f32 v179, v102, v103\n"
      "v_fma_f32 v107, v107, s54, -v14\n"
      "v_exp_f32_e64 v106, v106\n"
      "v_mfma_f32_32x32x16_bf16 v[112:127], a[116:119], a[52:55], v[112:127]\n"
      "ds_read_b128 a[84:87], v82 offset:16384\n"
      "s_add_u32 m0, s58, 49152\n"
      "s_mov_b32 s61, 196608\n"
      "v_add_f32_e64 v17, v17, v105\n"
      "v_fma_f32 v108, v108, s54, -v14\n"
      "v_exp_f32_e64 v107, v107\n"
      "v_mfma_f32_32x32x16_bf16 v[112:127], a[120:123], a[56:59], v[112:127]\n"
      "ds_read_b128 a[88:91], v83 offset:0\n"
      "buffer_load_dwordx4 v2, s[40:43], s61 offen offset:0 nt lds\n"
      "v_add_f32_e64 v16, v16, v106\n"
      "v_cvt_pk_bf16_f32 v180, v104, v105\n"
      "v_fma_f32 v109, v109, s54, -v14\n"
      "v_exp_f32_e64 v108, v108\n"
      "v_mfma_f32_32x32x16_bf16 v[112:127], a[124:127], a[60:63], v[112:127]\n"
      "ds_read_b128 a[92:95], v83 offset:16384\n"
      "v_add_f32_e64 v17, v17, v107\n"
      "v_fma_f32 v110, v110, s54, -v14\n"
      "v_exp_f32_e64 v109, v109\n"
      "v_add_f32 v16, v16, v108\n"
      "s_waitcnt lgkmcnt(4)\n"
      "v_mfma_f32_32x32x16_bf16 v[32:47], a[64:67], a[0:3], 0\n"
      "ds_read_b128 a[96:99], v84 offset:0\n"
      "buffer_load_dwordx4 v3, s[40:43], s61 offen offset:1024 nt lds\n"
      "v_cvt_pk_bf16_f32 v181, v106, v107\n"
      "v_fma_f32 v111, v111, s54, -v14\n"
      "v_exp_f32_e64 v110, v110\n"
      "v_mfma_f32_32x32x16_bf16 v[48:63], a[68:71], a[0:3], 0\n"
      "ds_read_b128 a[100:103], v84 offset:16384\n"
      "v_add_f32 v17, v17, v109\n"
      "v_exp_f32 v111, v111\n"
      "v_add_f32_e64 v16, v16, v110\n"
      "v_cvt_pk_bf16_f32 v182, v108, v109\n"
      "v_add_f32_e64 v17, v17, v111\n"
      "v_cvt_pk_bf16_f32 v183, v110, v111\n"
      "v_mfma_f32_32x32x16_bf16 v[32:47], a[72:75], a[4:7], v[32:47]\n"
      "ds_read_b128 a[104:107], v85 offset:0\n"
      "v_max3_f32 v18, v112, v113, v114\n"
      "v_max3_f32 v19, v119, v120, v121\n"
      "v_max3_f32 v18, v18, v115, v116\n"
      "v_max3_f32 v19, v19, v122, v123\n"
      "v_mfma_f32_32x32x16_bf16 v[48:63], a[76:79], a[4:7], v[48:63]\n"
      "ds_read_b128 a[108:111], v85 offset:16384\n"
      "buffer_load_dwordx4 v4, s[40:43], s61 offen offset:2048 nt lds\n"
      "v_max3_f32 v18, v18, v117, v118\n"
      "s_waitcnt lgkmcnt(4)\n"
      "s_nop 0\n"
      "v_mfma_f32_32x32x16_bf16 v[32:47], a[80:83], a[8:11], v[32:47]\n"
      "ds_read_b128 a[112:115], v86 offset:0\n"
      "v_max3_f32 v19, v19, v124, v125\n"
      "v_max3_f32 v19, v19, v126, v127\n"
      "v_max_f32_e64 v18, v18, v19\n"
      "v_mfma_f32_32x32x16_bf16 v[48:63], a[84:87], a[8:11], v[48:63]\n"
      "ds_read_b128 a[116:119], v86 offset:16384\n"
      "v_cmp_gt_f32 vcc, v18, v15\n"
      "s_cbranch_vccnz .Lrare6_%=\n"
      ".Lret6_%=:\n"
      "v_fma_f32 v112, v112, s54, -v14\n"
      "v_fma_f32 v113, v113, s54, -v14\n"
      "v_mfma_f32_32x32x16_bf16 v[32:47], a[88:91], a[12:15], v[32:47]\n"
      "ds_read_b128 a[120:123], v87 offset:0\n"
      "buffer_load_dwordx4 v5, s[40:43], s61 offen offset:3072 nt lds\n"
      "v_exp_f32_e64 v112, v112\n"
      "v_mfma_f32_32x32x16_bf16 v[48:63], a[92:95], a[12:15], v[48:63]\n"
      "ds_read_b128 a[124:127], v87 offset:16384\n"
      "v_fma_f32 v114, v114, s54, -v14\n"
      "v_exp_f32 v113, v113\n"
      "s_waitcnt lgkmcnt(4)\n"
      "v_mfma_f32_32x32x16_bf16 v[32:47], a[96:99], a[16:19], v[32:47]\n"
      "ds_read_b128 a[64:67], v80 offset:256\n"
      "v_add_f32_e64 v16, v16, v112\n"
      "v_fma_f32 v115, v115, s54, -v14\n"
      "v_exp_f32_e64 v114, v114\n"
      "v_mfma_f32_32x32x16_bf16 v[48:63], a[100:103], a[16:19], v[48:63]\n"
      "ds_read_b128 a[68:71], v80 offset:16640\n"
      "s_add_u32 m0, s58, 65536\n"
      "s_mov_b32 s61, 212992\n"
      "v_add_f32_e64 v17, v17, v113\n"
      "v_mfma_f32_32x32x16_bf16 v[32:47], a[104:107], a[20:23], v[32:47]\n"
      "ds_read_b128 a[72:75], v81 offset:256\n"
      "buffer_load_dwordx4 v2, s[40:43], s61 offen offset:0 nt lds\n"
      "v_fma_f32 v116, v116, s54, -v14\n"
      "v_exp_f32_e64 v115, v115\n"
      "v_mfma_f32_32x32x16_bf16 v[48:63], a[108:111], a[20:23], v[48:63]\n"
      "ds_read_b128 a[76:79], v81 offset:16640\n"
      "v_add_f32 v16, v16, v114\n"
      "s_waitcnt lgkmcnt(4)\n"
      "v_cvt_pk_bf16_f32 v184, v112, v113\n"
      "v_mfma_f32_32x32x16_bf16 v[32:47], a[112:115], a[24:27], v[32:47]\n"
      "ds_read_b128 a[80:83], v82 offset:256\n"
      "buffer_load_dwordx4 v3, s[40:43], s61 offen offset:1024 nt lds\n"
      "v_fma_f32 v117, v117, s54, -v14\n"
      "v_mfma_f32_32x32x16_bf16 v[48:63], a[116:119], a[24:27], v[48:63]\n"
      "ds_read_b128 a[84:87], v82 offset:16640\n"
      "v_exp_f32 v116, v116\n"
      "v_add_f32 v17, v17, v115\n"
      "v_fma_f32 v118, v118, s54, -v14\n"
      "v_mfma_f32_32x32x16_bf16 v[32:47], a[120:123], a[28:31], v[32:47]\n"
      "ds_read_b128 a[88:91], v83 offset:256\n"
      "v_exp_f32 v117, v117\n"
      "v_add_f32 v16, v16, v116\n"
      "v_cvt_pk_bf16_f32 v185, v114, v115\n"
      "v_mfma_f32_32x32x16_bf16 v[48:63], a[124:127], a[28:31], v[48:63]\n"
      "ds_read_b128 a[92:95], v83 offset:16640\n"
      "buffer_load_dwordx4 v4, s[40:43], s61 offen offset:2048 nt lds\n"
      "v_fma_f32 v119, v119, s54, -v14\n"
      "s_waitcnt lgkmcnt(4)\n"
      "s_nop 0\n"
      "v_mfma_f32_32x32x16_bf16 v[32:47], a[64:67], a[32:35], v[32:47]\n"
      "ds_read_b128 a[96:99], v84 offset:256\n"
      "v_exp_f32 v118, v118\n"
      "v_add_f32 v17, v17, v117\n"
      "v_fma_f32 v120, v120, s54, -v14\n"
      "v_mfma_f32_32x32x16_bf16 v[48:63], a[68:71], a[32:35], v[48:63]\n"
      "ds_read_b128 a[100:103], v84 offset:16640\n"
      "v_exp_f32 v119, v119\n"
      "v_add_f32 v16, v16, v118\n"
      "v_cvt_pk_bf16_f32 v186, v116, v117\n"
      "v_mfma_f32_32x32x16_bf16 v[32:47], a[72:75], a[36:39], v[32:47]\n"
      "ds_read_b128 a[104:107], v85 offset:256\n"
      "buffer_load_dwordx4 v5, s[40:43], s61 offen offset:3072 nt lds\n"
      "v_fma_f32 v121, v121, s54, -v14\n"
      "v_mfma_f32_32x32x16_bf16 v[48:63], a[76:79], a[36:39], v[48:63]\n"
      "ds_read_b128 a[108:111], v85 offset:16640\n"
      "v_exp_f32_e64 v120, v120\n"
      "v_add_f32 v17, v17, v119\n"
      "s_waitcnt lgkmcnt(4)\n"
      "v_mfma_f32_32x32x16_bf16 v[32:47], a[80:83], a[40:43], v[32:47]\n"
      "ds_read_b128 a[112:115], v86 offset:256\n"
      "v_fma_f32 v122, v122, s54, -v14\n"
      "v_exp_f32 v121, v121\n"
      "v_add_f32 v16, v16, v120\n"
      "v_mfma_f32_32x32x16_bf16 v[48:63], a[84:87], a[40:43], v[48:63]\n"
      "ds_read_b128 a[116:119], v86 offset:16640\n"
      "s_add_u32 m0, s58, 81920\n"
      "s_mov_b32 s61, 65536\n"
      "v_cvt_pk_bf16_f32 v187, v118, v119\n"
      "v_fma_f32 v123, v123, s54, -v14\n"
      "v_mfma_f32_32x32x16_bf16 v[32:47], a[88:91], a[44:47], v[32:47]\n"
      "ds_read_b128 a[120:123], v87 offset:256\n"
      "buffer_load_dwordx4 v6, s[44:47], s61 offen offset:0 nt lds\n"
      "v_exp_f32_e64 v122, v122\n"
      "v_mfma_f32_32x32x16_bf16 v[48:63], a[92:95], a[44:47], v[48:63]\n"
      "ds_read_b128 a[124:127], v87 offset:16640\n"
      "v_add_f32 v17, v17, v121\n"
      "s_waitcnt lgkmcnt(4)\n"
      "v_fma_f32 v124, v124, s54, -v14\n"
      "v_mfma_f32_32x32x16_bf16 v[32:47], a[96:99], a[48:51], v[32:47]\n"
      "v_exp_f32 v123, v123\n"
      "v_add_f32 v16, v16, v122\n"
      "v_cvt_pk_bf16_f32 v188, v120, v121\n"
      "v_fma_f32 v125, v125, s54, -v14\n"
      "v_mfma_f32_32x32x16_bf16 v[48:63], a[100:103], a[48:51], v[48:63]\n"
      "buffer_load_dwordx4 v7, s[44:47], s61 offen offset:1024 nt lds\n"
      "v_exp_f32 v124, v124\n"
      "v_add_f32 v17, v17, v123\n"
      "v_mfma_f32_32x32x16_bf16 v[32:47], a[104:107], a[52:55], v[32:47]\n"
      "v_fma_f32 v126, v126, s54, -v14\n"
      "v_exp_f32 v125, v125\n"
      "v_add_f32 v16, v16, v124\n"
      "v_cvt_pk_bf16_f32 v189, v122, v123\n"
      "v_mfma_f32_32x32x16_bf16 v[48:63], a[108:111], a[52:55], v[48:63]\n"
      "s_waitcnt lgkmcnt(0)\n"
      "s_waitcnt vmcnt(10)\n"
      "s_barrier\n"
      "s_nop 0\n"
      "v_mfma_f32_32x32x16_bf16 v[32:47], a[112:115], a[56:59], v[32:47]\n"
      "ds_read_b128 a[64:67], v64 offset:0\n"
      "ds_read_b128 a[68:71], v64 offset:16384\n"
      "ds_read_b64_tr_b16 a[72:73], v10 offset:32768\n"
      "ds_read_b64_tr_b16 a[74:75], v10 offset:33792\n"
      "ds_read_b128 a[76:79], v65 offset:0\n"
      "ds_read_b128 a[80:83], v65 offset:16384\n"
      "buffer_load_dwordx4 v8, s[44:47], s61 offen offset:2048 nt lds\n"
      "v_mfma_f32_32x32x16_bf16 v[48:63], a[116:119], a[56:59], v[48:63]\n"
      "ds_read_b64_tr_b16 a[84:85], v11 offset:32768\n"
      "ds_read_b64_tr_b16 a[86:87], v11 offset:33792\n"
      "v_fma_f32 v127, v127, s54, -v14\n"
      "v_exp_f32_e64 v126, v126\n"
      "v_mfma_f32_32x32x16_bf16 v[32:47], a[120:123], a[60:63], v[32:47]\n"
      "ds_read_b128 a[88:91], v66 offset:0\n"
      "v_add_f32 v17, v17, v125\n"
      "v_exp_f32 v127, v127\n"
      "v_mfma_f32_32x32x16_bf16 v[48:63], a[124:127], a[60:63], v[48:63]\n"
      "ds_read_b128 a[92:95], v66 offset:16384\n"
      "buffer_load_dwordx4 v9, s[44:47], s61 offen offset:3072 nt lds\n"
      "v_add_f32 v16, v16, v126\n"
      ".Ljoin_%=:\n"
      "s_waitcnt lgkmcnt(5)\n"
      "v_mfma_f32_32x32x16_bf16 v[96:111], a[64:67], a[0:3], 0\n"
      "ds_read_b64_tr_b16 a[96:97], v10 offset:34816\n"
      "ds_read_b64_tr_b16 a[98:99], v10 offset:35840\n"
      "v_cvt_pk_bf16_f32 v190, v124, v125\n"
      "v_add_f32_e64 v17, v17, v127\n"
      "v_cvt_pk_bf16_f32 v191, v126, v127\n"
      "v_mfma_f32_32x32x16_bf16 v[112:127], a[68:71], a[0:3], 0\n"
      "ds_read_b128 a[100:103], v67 offset:0\n"
      "v_mfma_f32_32x32x16_bf16 a[128:143], a[72:75], v[128:131], 0\n"
      "ds_read_b128 a[104:107], v67 offset:16384\n"
      "s_add_u32 m0, s58, 98304\n"
      "s_mov_b32 s61, 229376\n"
      "v_max3_f32 v18, v32, v33, v34\n"
      "v_max3_f32 v19, v47, v48, v49\n"
      "v_max3_f32 v18, v18, v35, v36\n"
      "v_mfma_f32_32x32x16_bf16 v[96:111], a[76:79], a[4:7], v[96:111]\n"
      "ds_read_b64_tr_b16 a[108:109], v11 offset:34816\n"
      "ds_read_b64_tr_b16 a[110:111], v11 offset:35840\n"
      "buffer_load_dwordx4 v2, s[40:43], s61 offen offset:0 nt lds\n"
      "v_max3_f32 v19, v19, v50, v51\n"
      "s_waitcnt lgkmcnt(6)\n"
      "s_nop 0\n"
      "v_mfma_f32_32x32x16_bf16 v[112:127], a[80:83], a[4:7], v[112:127]\n"
      "ds_read_b128 a[112:115], v68 offset:0\n"
      "v_max3_f32 v18, v18, v37, v38\n"
      "v_max3_f32 v19, v19, v52, v53\n"
      "v_max3_f32 v18, v18, v39, v40\n"
      "v_max3_f32 v19, v19, v54, v55\n"
      "v_max3_f32 v18, v18, v41, v42\n"
      "v_mfma_f32_32x32x16_bf16 a[144:159], a[84:87], v[128:131], 0\n"
      "ds_read_b128 a[116:119], v68 offset:16384\n"
      "v_max3_f32 v19, v19, v56, v57\n"
      "v_max3_f32 v18, v18, v43, v44\n"
      "v_max3_f32 v19, v19, v58, v59\n"
      "v_max3_f32 v18, v18, v45, v46\n"
      "v_mfma_f32_32x32x16_bf16 v[96:111], a[88:91], a[8:11], v[96:111]\n"
      "ds_read_b64_tr_b16 a[120:121], v10 offset:36864\n"
      "ds_read_b64_tr_b16 a[122:123], v10 offset:37888\n"
      "buffer_load_dwordx4 v3, s[40:43], s61 offen offset:1024 nt lds\n"
      "v_max3_f32 v19, v19, v60, v61\n"
      "v_max3_f32 v19, v19, v62, v63\n"
      "v_mfma_f32_32x32x16_bf16 v[112:127], a[92:95], a[8:11], v[112:127]\n"
      "ds_read_b128 a[124:127], v69 offset:0\n"
      "v_max_f32_e64 v18, v18, v19\n"
      "v_cmp_gt_f32 vcc, v18, v15\n"
      "s_cbranch_vccnz .Lrare7_%=\n"
      ".Lret7_%=:\n"
      "v_fma_f32 v32, v32, s54, -v14\n"
      "s_waitcnt lgkmcnt(5)\n"
      "s_nop 0\n"
      "v_mfma_f32_32x32x16_bf16 a[128:143], a[96:99], v[132:135], a[128:143]\n"
      "ds_read_b128 a[64:67], v69 offset:16384\n"
      "v_fma_f32 v33, v33, s54, -v14\n"
      "v_exp_f32_e64 v32, v32\n"
      "v_fma_f32 v34, v34, s54, -v14\n"
      "v_exp_f32_e64 v33, v33\n"
      "v_mfma_f32_32x32x16_bf16 v[96:111], a[100:103], a[12:15], v[96:111]\n"
      "ds_read_b64_tr_b16 a[68:69], v11 offset:36864\n"
      "ds_read_b64_tr_b16 a[70:71], v11 offset:37888\n"
      "buffer_load_dwordx4 v4, s[40:43], s61 offen offset:2048 nt lds\n"
      "v_add_f32_e64 v16, v16, v32\n"
      "v_mfma_f32_32x32x16_bf16 v[112:127], a[104:107], a[12:15], v[112:127]\n"
      "ds_read_b128 a[72:75], v70 offset:0\n"
      "v_fma_f32 v35, v35, s54, -v14\n"
      "v_exp_f32 v34, v34\n"
      "v_add_f32 v17, v17, v33\n"
      "v_fma_f32 v36, v36, s54, -v14\n"
      "v_mfma_f32_32x32x16_bf16 a[144:159], a[108:111], v[132:135], a[144:159]\n"
      "ds_read_b128 a[76:79], v70 offset:16384\n"
      "v_exp_f32 v35, v35\n"
      "v_add_f32 v16, v16, v34\n"
      "v_cvt_pk_bf16_f32 v192, v32, v33\n"
      "s_waitcnt lgkmcnt(5)\n"
      "s_nop 0\n"
      "v_mfma_f32_32x32x16_bf16 v[96:111], a[112:115], a[16:19], v[96:111]\n"
      "ds_read_b64_tr_b16 a[80:81], v10 offset:38912\n"
      "ds_read_b64_tr_b16 a[82:83], v10 offset:39936\n"
      "buffer_load_dwordx4 v5, s[40:43], s61 offen offset:3072 nt lds\n"
      "v_fma_f32 v37, v37, s54, -v14\n"
      "v_mfma_f32_32x32x16_bf16 v[112:127], a[116:119], a[16:19], v[112:127]\n"
      "ds_read_b128 a[84:87], v71 offset:0\n"
      "v_exp_f32 v36, v36\n"
      "v_add_f32 v17, v17, v35\n"
      "v_fma_f32 v38, v38, s54, -v14\n"
      "v_exp_f32_e64 v37, v37\n"
      "v_mfma_f32_32x32x16_bf16 a[128:143], a[120:123], v[136:139], a[128:143]\n"
      "ds_read_b128 a[88:91], v71 offset:16384\n"
      "v_add_f32_e64 v16, v16, v36\n"
      "v_cvt_pk_bf16_f32 v193, v34, v35\n"
      "v_fma_f32 v39, v39, s54, -v14\n"
      "v_exp_f32_e64 v38, v38\n"
      "v_mfma_f32_32x32x16_bf16 v[96:111], a[124:127], a[20:23], v[96:111]\n"
      "ds_read_b64_tr_b16 a[92:93], v11 offset:38912\n"
      "ds_read_b64_tr_b16 a[94:95], v11 offset:39936\n"
      "v_add_f32 v17, v17, v37\n"
      "s_waitcnt lgkmcnt(6)\n"
      "v_fma_f32 v40, v40, s54, -v14\n"
      "v_mfma_f32_32x32x16_bf16 v[112:127], a[64:67], a[20:23], v[112:127]\n"
      "ds_read_b128 a[96:99], v64 offset:256\n"
      "s_add_u32 m0, s58, 114688\n"
      "s_mov_b32 s61, 245760\n"
      "v_exp_f32 v39, v39\n"
      "v_add_f32 v16, v16, v38\n"
      "v_mfma_f32_32x32x16_bf16 a[144:159], a[68:71], v[136:139], a[144:159]\n"
      "ds_read_b128 a[100:103], v64 offset:16640\n"
      "buffer_load_dwordx4 v2, s[40:43], s61 offen offset:0 nt lds\n"
      "v_cvt_pk_bf16_f32 v194, v36, v37\n"
      "v_fma_f32 v41, v41, s54, -v14\n"
      "v_mfma_f32_32x32x16_bf16 v[96:111], a[72:75], a[24:27], v[96:111]\n"
      "ds_read_b64_tr_b16 a[104:105], v10 offset:40960\n"
      "ds_read_b64_tr_b16 a[106:107], v10 offset:41984\n"
      "v_exp_f32 v40, v40\n"
      "v_add_f32 v17, v17, v39\n"
      "v_fma_f32 v42, v42, s54, -v14\n"
      "v_mfma_f32_32x32x16_bf16 v[112:127], a[76:79], a[24:27], v[112:127]\n"
      "ds_read_b128 a[108:111], v65 offset:256\n"
      "v_exp_f32 v41, v41\n"
      "v_add_f32 v16, v16, v40\n"
      "v_cvt_pk_bf16_f32 v195, v38, v39\n"
      "v_fma_f32 v43, v43, s54, -v14\n"
      "s_waitcnt lgkmcnt(5)\n"
      "s_nop 0\n"
      "v_mfma_f32_32x32x16_bf16 a[128:143], a[80:83], v[140:143], a[128:143]\n"
      "ds_read_b128 a[112:115], v65 offset:16640\n"
      "buffer_load_dwordx4 v3, s[40:43], s61 offen offset:1024 nt lds\n"
      "v_exp_f32_e64 v42, v42\n"
      "v_mfma_f32_32x32x16_bf16 v[96:111], a[84:87], a[28:31], v[96:111]\n"
      "ds_read_b64_tr_b16 a[116:117], v11 offset:40960\n"
      "ds_read_b64_tr_b16 a[118:119], v11 offset:41984\n"
      "v_add_f32_e64 v17, v17, v41\n"
      "v_fma_f32 v44, v44, s54, -v14\n"
      "v_exp_f32_e64 v43, v43\n"
      "v_mfma_f32_32x32x16_bf16 v[112:127], a[88:91], a[28:31], v[112:127]\n"
      "ds_read_b128 a[120:123], v66 offset:256\n"
      "v_add_f32_e64 v16, v16, v42\n"
      "v_cvt_pk_bf16_f32 v196, v40, v41\n"
      "v_fma_f32 v45, v45, s54, -v14\n"
      "v_exp_f32_e64 v44, v44\n"
      "v_mfma_f32_32x32x16_bf16 a[144:159], a[92:95], v[140:143], a[144:159]\n"
      "ds_read_b128 a[124:127], v66 offset:16640\n"
      "buffer_load_dwordx4 v4, s[40:43], s61 offen offset:2048 nt lds\n"
      "v_add_f32 v17, v17, v43\n"
      "s_waitcnt lgkmcnt(5)\n"
      "v_fma_f32 v46, v46, s54, -v14\n"
      "v_mfma_f32_32x32x16_bf16 v[96:111], a[96:99], a[32:35], v[96:111]\n"
      "ds_read_b64_tr_b16 a[64:65], v10 offset:43008\n"
      "ds_read_b64_tr_b16 a[66:67], v10 offset:44032\n"
      "v_exp_f32 v45, v45\n"
      "v_add_f32 v16, v16, v44\n"
      "v_cvt_pk_bf16_f32 v197, v42, v43\n"
      "v_mfma_f32_32x32x16_bf16 v[112:127], a[100:103], a[32:35], v[112:127]\n"
      "ds_read_b128 a[68:71], v67 offset:256\n"
      "v_fma_f32 v47, v47, s54, -v14\n"
      "v_exp_f32 v46, v46\n"
      "v_add_f32 v17, v17, v45\n"
      "v_fma_f32 v48, v48, s54, -v14\n"
      "v_mfma_f32_32x32x16_bf16 a[128:143], a[104:107], v[144:147], a[128:143]\n"
      "ds_read_b128 a[72:75], v67 offset:16640\n"
      "buffer_load_dwordx4 v5, s[40:43], s61 offen offset:3072 nt lds\n"
      "v_exp_f32 v47, v47\n"
      "v_add_f32 v16, v16, v46\n"
      "v_mfma_f32_32x32x16_bf16 v[96:111], a[108:111], a[36:39], v[96:111]\n"
      "ds_read_b64_tr_b16 a[76:77], v11 offset:43008\n"
      "ds_read_b64_tr_b16 a[78:79], v11 offset:44032\n"
      "v_cvt_pk_bf16_f32 v198, v44, v45\n"
      "v_fma_f32 v49, v49, s54, -v14\n"
      "s_waitcnt lgkmcnt(6)\n"
      "s_nop 0\n"
      "v_mfma_f32_32x32x16_bf16 v[112:127], a[112:115], a[36:39], v[112:127]\n"
      "ds_read_b128 a[80:83], v68 offset:256\n"
      "v_exp_f32 v48, v48\n"
      "v_add_f32 v17, v17, v47\n"
      "v_fma_f32 v50, v50, s54, -v14\n"
      "v_exp_f32_e64 v49, v49\n"
      "v_mfma_f32_32x32x16_bf16 a[144:159], a[116:119], v[144:147], a[144:159]\n"
      "ds_read_b128 a[84:87], v68 offset:16640\n"
      "v_add_f32_e64 v16, v16, v48\n"
      "v_cvt_pk_bf16_f32 v199, v46, v47\n"
      "v_fma_f32 v51, v51, s54, -v14\n"
      "v_exp_f32_e64 v50, v50\n"
      "v_mfma_f32_32x32x16_bf16 v[96:111], a[120:123], a[40:43], v[96:111]\n"
      "ds_read_b64_tr_b16 a[88:89], v10 offset:45056\n"
      "ds_read_b64_tr_b16 a[90:91], v10 offset:46080\n"
      "s_add_u32 m0, s58, 131072\n"
      "s_mov_b32 s61, 131072\n"
      "v_add_f32_e64 v17, v17, v49\n"
      "v_mfma_f32_32x32x16_bf16 v[112:127], a[124:127], a[40:43], v[112:127]\n"
      "ds_read_b128 a[92:95], v69 offset:256\n"
      "buffer_load_dwordx4 v6, s[44:47], s61 offen offset:0 nt lds\n"
      "v_fma_f32 v52, v52, s54, -v14\n"
      "v_exp_f32 v51, v51\n"
      "s_waitcnt lgkmcnt(5)\n"
      "v_mfma_f32_32x32x16_bf16 a[128:143], a[64:67], v[148:151], a[128:143]\n"
      "ds_read_b128 a[96:99], v69 offset:16640\n"
      "v_add_f32_e64 v16, v16, v50\n"
      "v_cvt_pk_bf16_f32 v200, v48, v49\n"
      "v_fma_f32 v53, v53, s54, -v14\n"
      "v_mfma_f32_32x32x16_bf16 v[96:111], a[68:71], a[44:47], v[96:111]\n"
      "ds_read_b64_tr_b16 a[100:101], v11 offset:45056\n"
      "ds_read_b64_tr_b16 a[102:103], v11 offset:46080\n"
      "v_exp_f32 v52, v52\n"
      "v_add_f32 v17, v17, v51\n"
      "v_fma_f32 v54, v54, s54, -v14\n"
      "v_mfma_f32_32x32x16_bf16 v[112:127], a[72:75], a[44:47], v[112:127]\n"
      "ds_read_b128 a[104:107], v70 offset:256\n"
      "buffer_load_dwordx4 v7, s[44:47], s61 offen offset:1024 nt lds\n"
      "v_exp_f32 v53, v53\n"
      "v_add_f32 v16, v16, v52\n"
      "v_cvt_pk_bf16_f32 v201, v50, v51\n"
      "v_mfma_f32_32x32x16_bf16 a[144:159], a[76:79], v[148:151], a[144:159]\n"
      "ds_read_b128 a[108:111], v70 offset:16640\n"
      "v_fma_f32 v55, v55, s54, -v14\n"
      "v_exp_f32 v54, v54\n"
      "s_waitcnt lgkmcnt(5)\n"
      "v_mfma_f32_32x32x16_bf16 v[96:111], a[80:83], a[48:51], v[96:111]\n"
      "ds_read_b64_tr_b16 a[112:113], v10 offset:47104\n"
      "ds_read_b64_tr_b16 a[114:115], v10 offset:48128\n"
      "v_add_f32_e64 v17, v17, v53\n"
      "v_fma_f32 v56, v56, s54, -v14\n"
      "v_exp_f32_e64 v55, v55\n"
      "v_mfma_f32_32x32x16_bf16 v[112:127], a[84:87], a[48:51], v[112:127]\n"
      "ds_read_b128 a[116:119], v71 offset:256\n"
      "buffer_load_dwordx4 v8, s[44:47], s61 offen offset:2048 nt lds\n"
      "v_add_f32_e64 v16, v16, v54\n"
      "v_cvt_pk_bf16_f32 v202, v52, v53\n"
      "v_fma_f32 v57, v57, s54, -v14\n"
      "v_mfma_f32_32x32x16_bf16 a[128:143], a[88:91], v[152:155], a[128:143]\n"
      "ds_read_b128 a[120:123], v71 offset:16640\n"
      "v_exp_f32 v56, v56\n"
      "v_add_f32 v17, v17, v55\n"
      "v_fma_f32 v58, v58, s54, -v14\n"
      "v_exp_f32_e64 v57, v57\n"
      "v_mfma_f32_32x32x16_bf16 v[96:111], a[92:95], a[52:55], v[96:111]\n"
      "ds_read_b64_tr_b16 a[124:125], v11 offset:47104\n"
      "ds_read_b64_tr_b16 a[126:127], v11 offset:48128\n"
      "v_add_f32 v16, v16, v56\n"
      "s_waitcnt lgkmcnt(6)\n"
      "v_cvt_pk_bf16_f32 v203, v54, v55\n"
      "v_mfma_f32_32x32x16_bf16 v[112:127], a[96:99], a[52:55], v[112:127]\n"
      "buffer_load_dwordx4 v9, s[44:47], s61 offen offset:3072 nt lds\n"
      "v_fma_f32 v59, v59, s54, -v14\n"
      "v_exp_f32 v58, v58\n"
      "v_add_f32 v17, v17, v57\n"
      "v_mfma_f32_32x32x16_bf16 a[144:159], a[100:103], v[152:155], a[144:159]\n"
      "v_fma_f32 v60, v60, s54, -v14\n"
      "v_exp_f32 v59, v59\n"
      "v_add_f32 v16, v16, v58\n"
      "v_cvt_pk_bf16_f32 v204, v56, v57\n"
      "v_fma_f32 v61, v61, s54, -v14\n"
      "v_mfma_f32_32x32x16_bf16 v[96:111], a[104:107], a[56:59], v[96:111]\n"
      "v_exp_f32 v60, v60\n"
      "v_add_f32 v17, v17, v59\n"
      "v_fma_f32 v62, v62, s54, -v14\n"
      "v_exp_f32_e64 v61, v61\n"
      "v_mfma_f32_32x32x16_bf16 v[112:127], a[108:111], a[56:59], v[112:127]\n"
      "s_waitcnt lgkmcnt(0)\n"
      "s_waitcnt vmcnt(12)\n"
      "s_barrier\n"
      "s_nop 0\n"
      "v_mfma_f32_32x32x16_bf16 a[128:143], a[112:115], v[156:159], a[128:143]\n"
      "ds_read_b128 a[64:67], v64 offset:49152\n"
      "ds_read_b128 a[68:71], v72 offset:0\n"
      "ds_read_b64_tr_b16 a[72:73], v12 offset:16384\n"
      "ds_read_b64_tr_b16 a[74:75], v12 offset:17408\n"
      "ds_read_b128 a[76:79], v65 offset:49152\n"
      "ds_read_b128 a[80:83], v73 offset:0\n"
      "s_add_u32 m0, s58, 147456\n"
      "s_mov_b32 s61, 128\n"
      "v_mfma_f32_32x32x16_bf16 v[96:111], a[116:119], a[60:63], v[96:111]\n"
      "ds_read_b64_tr_b16 a[84:85], v13 offset:16384\n"
      "ds_read_b64_tr_b16 a[86:87], v13 offset:17408\n"
      "buffer_load_dwordx4 v6, s[44:47], s61 offen offset:0 nt lds\n"
      "v_add_f32_e64 v16, v16, v60\n"
      "v_mfma_f32_32x32x16_bf16 v[112:127], a[120:123], a[60:63], v[112:127]\n"
      "ds_read_b128 a[88:91], v66 offset:49152\n"
      "v_cvt_pk_bf16_f32 v205, v58, v59\n"
      "v_fma_f32 v63, v63, s54, -v14\n"
      "v_exp_f32 v62, v62\n"
      "v_add_f32 v17, v17, v61\n"
      "v_mfma_f32_32x32x16_bf16 a[144:159], a[124:127], v[156:159], a[144:159]\n"
      "ds_read_b128 a[92:95], v74 offset:0\n"
      "v_exp_f32 v63, v63\n"
      "v_add_f32 v16, v16, v62\n"
      "v_cvt_pk_bf16_f32 v206, v60, v61\n"
      "s_waitcnt lgkmcnt(5)\n"
      "s_nop 0\n"
      "v_mfma_f32_32x32x16_bf16 v[32:47], a[64:67], a[0:3], 0\n"
      "ds_read_b64_tr_b16 a[96:97], v12 offset:18432\n"
      "ds_read_b64_tr_b16 a[98:99], v12 offset:19456\n"
      "buffer_load_dwordx4 v7, s[44:47], s61 offen offset:1024 nt lds\n"
      "v_add_f32_e64 v17, v17, v63\n"
      "v_cvt_pk_bf16_f32 v207, v62, v63\n"
      "v_mfma_f32_32x32x16_bf16 v[48:63], a[68:71], a[0:3], 0\n"
      "ds_read_b128 a[100:103], v67 offset:49152\n"
      "v_max3_f32 v18, v96, v97, v98\n"
      "v_max3_f32 v19, v111, v112, v113\n"
      "v_max3_f32 v18, v18, v99, v100\n"
      "v_max3_f32 v19, v19, v114, v115\n"
      "v_max3_f32 v18, v18, v101, v102\n"
      "v_mfma_f32_32x32x16_bf16 a[128:143], a[72:75], v[160:163], a[128:143]\n"
      "ds_read_b128 a[104:107], v75 offset:0\n"
      "v_max3_f32 v19, v19, v116, v117\n"
      "v_max3_f32 v18, v18, v103, v104\n"
      "v_max3_f32 v19, v19, v118, v119\n"
      "v_max3_f32 v18, v18, v105, v106\n"
      "v_max3_f32 v19, v19, v120, v121\n"
      "v_mfma_f32_32x32x16_bf16 v[32:47], a[76:79], a[4:7], v[32:47]\n"
      "ds_read_b64_tr_b16 a[108:109], v13 offset:18432\n"
      "ds_read_b64_tr_b16 a[110:111], v13 offset:19456\n"
      "buffer_load_dwordx4 v8, s[44:47], s61 offen offset:2048 nt lds\n"
      "s_waitcnt lgkmcnt(6)\n"
      "s_nop 0\n"
      "v_mfma_f32_32x32x16_bf16 v[48:63], a[80:83], a[4:7], v[48:63]\n"
      "ds_read_b128 a[112:115], v68 offset:49152\n"
      "v_max3_f32 v18, v18, v107, v108\n"
      "v_max3_f32 v19, v19, v122, v123\n"
      "v_max3_f32 v18, v18, v109, v110\n"
      "v_max3_f32 v19, v19, v124, v125\n"
      "v_max3_f32 v19, v19, v126, v127\n"
      "v_mfma_f32_32x32x16_bf16 a[144:159], a[84:87], v[160:163], a[144:159]\n"
      "ds_read_b128 a[116:119], v76 offset:0\n"
      "v_max_f32_e64 v18, v18, v19\n"
      "v_cmp_gt_f32 vcc, v18, v15\n"
      "s_cbranch_vccnz .Lrare8_%=\n"
      ".Lret8_%=:\n"
      "v_fma_f32 v96, v96, s54, -v14\n"
      "v_fma_f32 v97, v97, s54, -v14\n"
      "v_mfma_f32_32x32x16_bf16 v[32:47], a[88:91], a[8:11], v[32:47]\n"
      "ds_read_b64_tr_b16 a[120:121], v12 offset:20480\n"
      "ds_read_b64_tr_b16 a[122:123], v12 offset:21504\n"
      "buffer_load_dwordx4 v9, s[44:47], s61 offen offset:3072 nt lds\n"
      "v_exp_f32_e64 v96, v96\n"
      "v_mfma_f32_32x32x16_bf16 v[48:63], a[92:95], a[8:11], v[48:63]\n"
      "ds_read_b128 a[124:127], v69 offset:49152\n"
      "v_fma_f32 v98, v98, s54, -v14\n"
      "v_exp_f32_e64 v97, v97\n"
      "v_add_f32 v16, v16, v96\n"
      "s_waitcnt lgkmcnt(5)\n"
      "v_mfma_f32_32x32x16_bf16 a[128:143], a[96:99], v[164:167], a[128:143]\n"
      "ds_read_b128 a[64:67], v77 offset:0\n"
      "v_fma_f32 v99, v99, s54, -v14\n"
      "v_exp_f32 v98, v98\n"
      "v_add_f32 v17, v17, v97\n"
      "v_mfma_f32_32x32x16_bf16 v[32:47], a[100:103], a[12:15], v[32:47]\n"
      "ds_read_b64_tr_b16 a[68:69], v13 offset:20480\n"
      "ds_read_b64_tr_b16 a[70:71], v13 offset:21504\n"
      "v_fma_f32 v100, v100, s54, -v14\n"
      "v_exp_f32 v99, v99\n"
      "v_add_f32 v16, v16, v98\n"
      "v_mfma_f32_32x32x16_bf16 v[48:63], a[104:107], a[12:15], v[48:63]\n"
      "ds_read_b128 a[72:75], v70 offset:49152\n"
      "s_add_u32 m0, s58, 0\n"
      "s_nop 0\n"
      "s_mov_b32 s61, 65664\n"
      "v_cvt_pk_bf16_f32 v208, v96, v97\n"
      "v_fma_f32 v101, v101, s54, -v14\n"
      "v_exp_f32_e64 v100, v100\n"
      "v_mfma_f32_32x32x16_bf16 a[144:159], a[108:111], v[164:167], a[144:159]\n"
      ".Lpad2_%=:\n"
      "s_cbranch_execz .LpadretA_%=\n"
      "s_nop 0\n"
      "ds_read_b128 a[76:79], v78 offset:0\n"
      "buffer_load_dwordx4 v6, s[44:47], s61 offen offset:0 nt lds\n"
      "v_add_f32 v17, v17, v99\n"
      "s_waitcnt lgkmcnt(5)\n"
      "v_mfma_f32_32x32x16_bf16 v[32:47], a[112:115], a[16:19], v[32:47]\n"
      "ds_read_b64_tr_b16 a[80:81], v12 offset:22528\n"
      "ds_read_b64_tr_b16 a[82:83], v12 offset:23552\n"
      "v_fma_f32 v102, v102, s54, -v14\n"
      "v_exp_f32 v101, v101\n"
      "v_add_f32 v16, v16, v100\n"
      "v_mfma_f32_32x32x16_bf16 v[48:63], a[116:119], a[16:19], v[48:63]\n"
      "ds_read_b128 a[84:87], v71 offset:49152\n"
      "v_cvt_pk_bf16_f32 v209, v98, v99\n"
      "v_fma_f32 v103, v103, s54, -v14\n"
      "v_exp_f32_e64 v102, v102\n"
      "v_mfma_f32_32x32x16_bf16 a[128:143], a[120:123], v[168:171], a[128:143]\n"
      "ds_read_b128 a[88:91], v79 offset:0\n"
      "buffer_load_dwordx4 v7, s[44:47], s61 offen offset:1024 nt lds\n"
      "v_add_f32_e64 v17, v17, v101\n"
      "v_fma_f32 v104, v104, s54, -v14\n"
      "v_exp_f32_e64 v103, v103\n"
      "v_mfma_f32_32x32x16_bf16 v[32:47], a[124:127], a[20:23], v[32:47]\n"
      "ds_read_b64_tr_b16 a[92:93], v13 offset:22528\n"
      "ds_read_b64_tr_b16 a[94:95], v13 offset:23552\n"
      "v_add_f32 v16, v16, v102\n"
      "s_waitcnt lgkmcnt(6)\n"
      "v_cvt_pk_bf16_f32 v210, v100, v101\n"
      "v_mfma_f32_32x32x16_bf16 v[48:63], a[64:67], a[20:23], v[48:63]\n"
      "ds_read_b128 a[96:99], v64 offset:49408\n"
      "v_fma_f32 v105, v105, s54, -v14\n"
      "v_exp_f32 v104, v104\n"
      "v_add_f32 v17, v17, v103\n"
      "v_fma_f32 v106, v106, s54, -v14\n"
      "v_mfma_f32_32x32x16_bf16 a[144:159], a[68:71], v[168:171], a[144:159]\n"
      "ds_read_b128 a[100:103], v72 offset:256\n"
      "buffer_load_dwordx4 v8, s[44:47], s61 offen offset:2048 nt lds\n"
      "v_exp_f32 v105, v105\n"
      "v_add_f32 v16, v16, v104\n"
      "v_mfma_f32_32x32x16_bf16 v[32:47], a[72:75], a[24:27], v[32:47]\n"
      "ds_read_b64_tr_b16 a[104:105], v12 offset:24576\n"
      "ds_read_b64_tr_b16 a[106:107], v12 offset:25600\n"
      "v_cvt_pk_bf16_f32 v211, v102, v103\n"
      "v_fma_f32 v107, v107, s54, -v14\n"
      "v_exp_f32_e64 v106, v106\n"
      "v_mfma_f32_32x32x16_bf16 v[48:63], a[76:79], a[24:27], v[48:63]\n"
      "ds_read_b128 a[108:111], v65 offset:49408\n"
      "v_add_f32_e64 v17, v17, v105\n"
      "v_fma_f32 v108, v108, s54, -v14\n"
      "v_exp_f32 v107, v107\n"
      "s_waitcnt lgkmcnt(5)\n"
      "v_mfma_f32_32x32x16_bf16 a[128:143], a[80:83], v[172:175], a[128:143]\n"
      "ds_read_b128 a[112:115], v73 offset:256\n"
      "buffer_load_dwordx4 v9, s[44:47], s61 offen offset:3072 nt lds\n"
      "v_add_f32_e64 v16, v16, v106\n"
      "v_cvt_pk_bf16_f32 v212, v104, v105\n"
      "v_mfma_f32_32x32x16_bf16 v[32:47], a[84:87], a[28:31], v[32:47]\n"
      "ds_read_b64_tr_b16 a[116:117], v13 offset:24576\n"
      "ds_read_b64_tr_b16 a[118:119], v13 offset:25600\n"
      "v_fma_f32 v109, v109, s54, -v14\n"
      "v_exp_f32 v108, v108\n"
      "v_add_f32 v17, v17, v107\n"
      "v_mfma_f32_32x32x16_bf16 v[48:63], a[88:91], a[28:31], v[48:63]\n"
      "ds_read_b128 a[120:123], v66 offset:49408\n"
      "v_fma_f32 v110, v110, s54, -v14\n"
      "v_exp_f32 v109, v109\n"
      "v_add_f32 v16, v16, v108\n"
      "v_cvt_pk_bf16_f32 v213, v106, v107\n"
      "v_mfma_f32_32x32x16_bf16 a[144:159], a[92:95], v[172:175], a[144:159]\n"
      "ds_read_b128 a[124:127], v74 offset:256\n"
      "v_fma_f32 v111, v111, s54, -v14\n"
      "v_exp_f32_e64 v110, v110\n"
      "v_add_f32 v17, v17, v109\n"
      "s_waitcnt lgkmcnt(5)\n"
      "v_mfma_f32_32x32x16_bf16 v[32:47], a[96:99], a[32:35], v[32:47]\n"
      "ds_read_b64_tr_b16 a[64:65], v12 offset:26624\n"
      "ds_read_b64_tr_b16 a[66:67], v12 offset:27648\n"
      "s_add_u32 m0, s58, 16384\n"
      "s_mov_b32 s61, 131200\n"
      "v_fma_f32 v112, v112, s54, -v14\n"
      "v_mfma_f32_32x32x16_bf16 v[48:63], a[100:103], a[32:35], v[48:63]\n"
      "ds_read_b128 a[68:71], v67 offset:49408\n"
      "buffer_load_dwordx4 v6, s[44:47], s61 offen offset:0 nt lds\n"
      "v_exp_f32 v111, v111\n"
      "v_add_f32 v16, v16, v110\n"
      "v_mfma_f32_32x32x16_bf16 a[128:143], a[104:107], v[176:179], a[128:143]\n"
      "ds_read_b128 a[72:75], v75 offset:256\n"
      "v_cvt_pk_bf16_f32 v214, v108, v109\n"
      "v_fma_f32 v113, v113, s54, -v14\n"
      "v_exp_f32 v112, v112\n"
      "v_add_f32 v17, v17, v111\n"
      "v_mfma_f32_32x32x16_bf16 v[32:47], a[108:111], a[36:39], v[32:47]\n"
      "ds_read_b64_tr_b16 a[76:77], v13 offset:26624\n"
      "ds_read_b64_tr_b16 a[78:79], v13 offset:27648\n"
      "v_fma_f32 v114, v114, s54, -v14\n"
      "v_exp_f32 v113, v113\n"
      "s_waitcnt lgkmcnt(6)\n"
      "v_mfma_f32_32x32x16_bf16 v[48:63], a[112:115], a[36:39], v[48:63]\n"
      "ds_read_b128 a[80:83], v68 offset:49408\n"
      "buffer_load_dwordx4 v7, s[44:47], s61 offen offset:1024 nt lds\n"
      "v_add_f32_e64 v16, v16, v112\n"
      "v_cvt_pk_bf16_f32 v215, v110, v111\n"
      "v_fma_f32 v115, v115, s54, -v14\n"
      "v_mfma_f32_32x32x16_bf16 a[144:159], a[116:119], v[176:179], a[144:159]\n"
      "ds_read_b128 a[84:87], v76 offset:256\n"
      "v_exp_f32 v114, v114\n"
      "v_add_f32 v17, v17, v113\n"
      "v_fma_f32 v116, v116, s54, -v14\n"
      "v_mfma_f32_32x32x16_bf16 v[32:47], a[120:123], a[40:43], v[32:47]\n"
      "ds_read_b64_tr_b16 a[88:89], v12 offset:28672\n"
      "ds_read_b64_tr_b16 a[90:91], v12 offset:29696\n"
      "v_exp_f32 v115, v115\n"
      "v_add_f32 v16, v16, v114\n"
      "v_cvt_pk_bf16_f32 v216, v112, v113\n"
      "v_mfma_f32_32x32x16_bf16 v[48:63], a[124:127], a[40:43], v[48:63]\n"
      "ds_read_b128 a[92:95], v69 offset:49408\n"
      "buffer_load_dwordx4 v8, s[44:47], s61 offen offset:2048 nt lds\n"
      "v_fma_f32 v117, v117, s54, -v14\n"
      "s_waitcnt lgkmcnt(5)\n"
      "s_nop 0\n"
      "v_mfma_f32_32x32x16_bf16 a[128:143], a[64:67], v[180:183], a[128:143]\n"
      "ds_read_b128 a[96:99], v77 offset:256\n"
      "v_exp_f32 v116, v116\n"
      "v_add_f32 v17, v17, v115\n"
      "v_fma_f32 v118, v118, s54, -v14\n"
      "v_exp_f32_e64 v117, v117\n"
      "v_mfma_f32_32x32x16_bf16 v[32:47], a[68:71], a[44:47], v[32:47]\n"
      "ds_read_b64_tr_b16 a[100:101], v13 offset:28672\n"
      "ds_read_b64_tr_b16 a[102:103], v13 offset:29696\n"
      "v_add_f32_e64 v16, v16, v116\n"
      "v_cvt_pk_bf16_f32 v217, v114, v115\n"
      "v_fma_f32 v119, v119, s54, -v14\n"
      "v_mfma_f32_32x32x16_bf16 v[48:63], a[72:75], a[44:47], v[48:63]\n"
      "ds_read_b128 a[104:107], v70 offset:49408\n"
      "buffer_load_dwordx4 v9, s[44:47], s61 offen offset:3072 nt lds\n"
      "v_exp_f32 v118, v118\n"
      "v_add_f32 v17, v17, v117\n"
      "v_mfma_f32_32x32x16_bf16 a[144:159], a[76:79], v[180:183], a[144:159]\n"
      "ds_read_b128 a[108:111], v78 offset:256\n"
      "v_fma_f32 v120, v120, s54, -v14\n"
      "v_exp_f32_e64 v119, v119\n"
      "v_add_f32 v16, v16, v118\n"
      "s_waitcnt lgkmcnt(5)\n"
      "v_mfma_f32_32x32x16_bf16 v[32:47], a[80:83], a[48:51], v[32:47]\n"
      "ds_read_b64_tr_b16 a[112:113], v12 offset:30720\n"
      "ds_read_b64_tr_b16 a[114:115], v12 offset:31744\n"
      "v_cvt_pk_bf16_f32 v218, v116, v117\n"
      "v_fma_f32 v121, v121, s54, -v14\n"
      "v_exp_f32_e64 v120, v120\n"
      "v_mfma_f32_32x32x16_bf16 v[48:63], a[84:87], a[48:51], v[48:63]\n"
      "ds_read_b128 a[116:119], v71 offset:49408\n"
      "v_add_f32_e64 v17, v17, v119\n"
      "v_fma_f32 v122, v122, s54, -v14\n"
      "v_exp_f32 v121, v121\n"
      "v_add_f32 v16, v16, v120\n"
      "v_mfma_f32_32x32x16_bf16 a[128:143], a[88:91], v[184:187], a[128:143]\n"
      "ds_read_b128 a[120:123], v79 offset:256\n"
      "s_add_u32 m0, s58, 32768\n"
      "s_mov_b32 s61, 196608\n"
      "v_cvt_pk_bf16_f32 v219, v118, v119\n"
      "v_fma_f32 v123, v123, s54, -v14\n"
      "v_mfma_f32_32x32x16_bf16 v[32:47], a[92:95], a[52:55], v[32:47]\n"
      "ds_read_b64_tr_b16 a[124:125], v13 offset:30720\n"
      "ds_read_b64_tr_b16 a[126:127], v13 offset:31744\n"
      "buffer_load_dwordx4 v6, s[44:47], s61 offen offset:0 nt lds\n"
      "v_exp_f32 v122, v122\n"
      "s_waitcnt lgkmcnt(6)\n"
      "v_mfma_f32_32x32x16_bf16 v[48:63], a[96:99], a[52:55], v[48:63]\n"
      "v_add_f32_e64 v17, v17, v121\n"
      "v_fma_f32 v124, v124, s54, -v14\n"
      "v_exp_f32 v123, v123\n"
      "v_add_f32 v16, v16, v122\n"
      "v_mfma_f32_32x32x16_bf16 a[144:159], a[100:103], v[184:187], a[144:159]\n"
      "v_cvt_pk_bf16_f32 v220, v120, v121\n"
      "v_fma_f32 v125, v125, s54, -v14\n"
      "v_exp_f32 v124, v124\n"
      "v_add_f32 v17, v17, v123\n"
      "v_fma_f32 v126, v126, s54, -v14\n"
      "v_mfma_f32_32x32x16_bf16 v[32:47], a[104:107], a[56:59], v[32:47]\n"
      "buffer_load_dwordx4 v7, s[44:47], s61 offen offset:1024 nt lds\n"
      "v_or_b32 v2, 0x20000, v10\n"
      "v_or_b32 v3, 0x20000, v11\n"
      "v_exp_f32_e64 v125, v125\n"
      "v_mfma_f32_32x32x16_bf16 v[48:63], a[108:111], a[56:59], v[48:63]\n"
      "s_waitcnt lgkmcnt(0)\n"
      "s_waitcnt vmcnt(14)\n"
      "s_barrier\n"
      "s_nop 0\n"
      "v_mfma_f32_32x32x16_bf16 a[128:143], a[112:115], v[188:191], a[128:143]\n"
      "ds_read_b128 a[64:67], v72 offset:32768\n"
      "ds_read_b128 a[68:71], v72 offset:49152\n"
      "ds_read_b64_tr_b16 a[72:73], v2 offset:0\n"
      "ds_read_b64_tr_b16 a[74:75], v2 offset:1024\n"
      "ds_read_b128 a[76:79], v73 offset:32768\n"
      "ds_read_b128 a[80:83], v73 offset:49152\n"
      "v_mfma_f32_32x32x16_bf16 v[32:47], a[116:119], a[60:63], v[32:47]\n"
      "ds_read_b64_tr_b16 a[84:85], v3 offset:0\n"
      "ds_read_b64_tr_b16 a[86:87], v3 offset:1024\n"
      "buffer_load_dwordx4 v8, s[44:47], s61 offen offset:2048 nt lds\n"
      "v_add_f32_e64 v16, v16, v124\n"
      "v_cvt_pk_bf16_f32 v221, v122, v123\n"
      "v_mfma_f32_32x32x16_bf16 v[48:63], a[120:123], a[60:63], v[48:63]\n"
      "ds_read_b128 a[88:91], v74 offset:32768\n"
      "v_fma_f32 v127, v127, s54, -v14\n"
      "v_exp_f32 v126, v126\n"
      "v_add_f32 v17, v17, v125\n"
      "v_mfma_f32_32x32x16_bf16 a[144:159], a[124:127], v[188:191], a[144:159]\n"
      "ds_read_b128 a[92:95], v74 offset:49152\n"
      "v_exp_f32 v127, v127\n"
      "v_add_f32 v16, v16, v126\n"
      "v_cvt_pk_bf16_f32 v222, v124, v125\n"
      "s_waitcnt lgkmcnt(5)\n"
      "s_nop 0\n"
      "v_mfma_f32_32x32x16_bf16 v[96:111], a[64:67], a[0:3], 0\n"
      "ds_read_b64_tr_b16 a[96:97], v2 offset:2048\n"
      "ds_read_b64_tr_b16 a[98:99], v2 offset:3072\n"
      "buffer_load_dwordx4 v9, s[44:47], s61 offen offset:3072 nt lds\n"
      "v_add_f32_e64 v17, v17, v127\n"
      "v_cvt_pk_bf16_f32 v223, v126, v127\n"
      "v_mfma_f32_32x32x16_bf16 v[112:127], a[68:71], a[0:3], 0\n"
      "ds_read_b128 a[100:103], v75 offset:32768\n"
      "v_max3_f32 v18, v32, v33, v34\n"
      "v_max3_f32 v19, v47, v48, v49\n"
      "v_max3_f32 v18, v18, v35, v36\n"
      "v_max3_f32 v19, v19, v50, v51\n"
      "v_max3_f32 v18, v18, v37, v38\n"
      "v_mfma_f32_32x32x16_bf16 a[128:143], a[72:75], v[192:195], a[128:143]\n"
      "ds_read_b128 a[104:107], v75 offset:49152\n"
      "v_max3_f32 v19, v19, v52, v53\n"
      "v_max3_f32 v18, v18, v39, v40\n"
      "v_max3_f32 v19, v19, v54, v55\n"
      "v_max3_f32 v18, v18, v41, v42\n"
      "v_max3_f32 v19, v19, v56, v57\n"
      "v_mfma_f32_32x32x16_bf16 v[96:111], a[76:79], a[4:7], v[96:111]\n"
      "ds_read_b64_tr_b16 a[108:109], v3 offset:2048\n"
      "ds_read_b64_tr_b16 a[110:111], v3 offset:3072\n"
      "v_max3_f32 v18, v18, v43, v44\n"
      "v_max3_f32 v19, v19, v58, v59\n"
      "v_max3_f32 v18, v18, v45, v46\n"
      "s_waitcnt lgkmcnt(6)\n"
      "s_nop 0\n"
      "v_mfma_f32_32x32x16_bf16 v[112:127], a[80:83], a[4:7], v[112:127]\n"
      "ds_read_b128 a[112:115], v76 offset:32768\n"
      "s_add_u32 m0, s58, 49152\n"
      "s_mov_b32 s61, 196736\n"
      "v_max3_f32 v19, v19, v60, v61\n"
      "v_max3_f32 v19, v19, v62, v63\n"
      "v_max_f32_e64 v18, v18, v19\n"
      "v_mfma_f32_32x32x16_bf16 a[144:159], a[84:87], v[192:195], a[144:159]\n"
      "ds_read_b128 a[116:119], v76 offset:49152\n"
      "buffer_load_dwordx4 v6, s[44:47], s61 offen offset:0 nt lds\n"
      "v_cmp_gt_f32 vcc, v18, v15\n"
      "s_cbranch_vccnz .Lrare9_%=\n"
      ".Lret9_%=:\n"
      "v_mfma_f32_32x32x16_bf16 v[96:111], a[88:91], a[8:11], v[96:111]\n"
      "ds_read_b64_tr_b16 a[120:121], v2 offset:4096\n"
      "ds_read_b64_tr_b16 a[122:123], v2 offset:5120\n"
      "v_fma_f32 v32, v32, s54, -v14\n"
      "v_fma_f32 v33, v33, s54, -v14\n"
      "v_exp_f32_e64 v32, v32\n"
      "v_mfma_f32_32x32x16_bf16 v[112:127], a[92:95], a[8:11], v[112:127]\n"
      "ds_read_b128 a[124:127], v77 offset:32768\n"
      "v_fma_f32 v34, v34, s54, -v14\n"
      "v_exp_f32_e64 v33, v33\n"
      "v_add_f32 v16, v16, v32\n"
      "s_waitcnt lgkmcnt(5)\n"
      "v_mfma_f32_32x32x16_bf16 a[128:143], a[96:99], v[196:199], a[128:143]\n"
      "ds_read_b128 a[64:67], v77 offset:49152\n"
      "buffer_load_dwordx4 v7, s[44:47], s61 offen offset:1024 nt lds\n"
      "v_fma_f32 v35, v35, s54, -v14\n"
      "v_exp_f32_e64 v34, v34\n"
      "v_mfma_f32_32x32x16_bf16 v[96:111], a[100:103], a[12:15], v[96:111]\n"
      "ds_read_b64_tr_b16 a[68:69], v3 offset:4096\n"
      "ds_read_b64_tr_b16 a[70:71], v3 offset:5120\n"
      "v_add_f32_e64 v17, v17, v33\n"
      "v_fma_f32 v36, v36, s54, -v14\n"
      "v_exp_f32_e64 v35, v35\n"
      "v_mfma_f32_32x32x16_bf16 v[112:127], a[104:107], a[12:15], v[112:127]\n"
      "ds_read_b128 a[72:75], v78 offset:32768\n"
      "v_add_f32_e64 v16, v16, v34\n"
      "v_cvt_pk_bf16_f32 v224, v32, v33\n"
      "v_fma_f32 v37, v37, s54, -v14\n"
      "v_exp_f32_e64 v36, v36\n"
      "v_mfma_f32_32x32x16_bf16 a[144:159], a[108:111], v[196:199], a[144:159]\n"
      "ds_read_b128 a[76:79], v78 offset:49152\n"
      "buffer_load_dwordx4 v8, s[44:47], s61 offen offset:2048 nt lds\n"
      "v_add_f32 v17, v17, v35\n"
      "s_waitcnt lgkmcnt(5)\n"
      "v_fma_f32 v38, v38, s54, -v14\n"
      "v_mfma_f32_32x32x16_bf16 v[96:111], a[112:115], a[16:19], v[96:111]\n"
      "ds_read_b64_tr_b16 a[80:81], v2 offset:6144\n"
      "ds_read_b64_tr_b16 a[82:83], v2 offset:7168\n"
      "v_exp_f32 v37, v37\n"
      "v_add_f32 v16, v16, v36\n"
      "v_cvt_pk_bf16_f32 v225, v34, v35\n"
      "v_mfma_f32_32x32x16_bf16 v[112:127], a[116:119], a[16:19], v[112:127]\n"
      "ds_read_b128 a[84:87], v79 offset:32768\n"
      "v_fma_f32 v39, v39, s54, -v14\n"
      "v_exp_f32 v38, v38\n"
      "v_add_f32 v17, v17, v37\n"
      "v_fma_f32 v40, v40, s54, -v14\n"
      "v_mfma_f32_32x32x16_bf16 a[128:143], a[120:123], v[200:203], a[128:143]\n"
      "ds_read_b128 a[88:91], v79 offset:49152\n"
      "buffer_load_dwordx4 v9, s[44:47], s61 offen offset:3072 nt lds\n"
      "v_exp_f32_e64 v39, v39\n"
      "v_mfma_f32_32x32x16_bf16 v[96:111], a[124:127], a[20:23], v[96:111]\n"
      "ds_read_b64_tr_b16 a[92:93], v3 offset:6144\n"
      "ds_read_b64_tr_b16 a[94:95], v3 offset:7168\n"
      "v_add_f32 v16, v16, v38\n"
      "s_waitcnt lgkmcnt(6)\n"
      "v_cvt_pk_bf16_f32 v226, v36, v37\n"
      "v_fma_f32 v41, v41, s54, -v14\n"
      "v_mfma_f32_32x32x16_bf16 v[112:127], a[64:67], a[20:23], v[112:127]\n"
      "ds_read_b128 a[96:99], v72 offset:33024\n"
      "v_exp_f32 v40, v40\n"
      "v_add_f32 v17, v17, v39\n"
      "v_fma_f32 v42, v42, s54, -v14\n"
      "v_exp_f32_e64 v41, v41\n"
      "v_mfma_f32_32x32x16_bf16 a[144:159], a[68:71], v[200:203], a[144:159]\n"
      "ds_read_b128 a[100:103], v72 offset:49408\n"
      "v_add_f32_e64 v16, v16, v40\n"
      "v_cvt_pk_bf16_f32 v227, v38, v39\n"
      "v_fma_f32 v43, v43, s54, -v14\n"
      "v_exp_f32_e64 v42, v42\n"
      "v_mfma_f32_32x32x16_bf16 v[96:111], a[72:75], a[24:27], v[96:111]\n"
      "ds_read_b64_tr_b16 a[104:105], v2 offset:8192\n"
      "ds_read_b64_tr_b16 a[106:107], v2 offset:9216\n"
      "s_add_u32 m0, s58, 65536\n"
      "s_mov_b32 s61, 256\n"
      "v_add_f32_e64 v17, v17, v41\n"
      "v_mfma_f32_32x32x16_bf16 v[112:127], a[76:79], a[24:27], v[112:127]\n"
      "ds_read_b128 a[108:111], v73 offset:33024\n"
      "buffer_load_dwordx4 v6, s[44:47], s61 offen offset:0 nt lds\n"
      "v_fma_f32 v44, v44, s54, -v14\n"
      "s_waitcnt lgkmcnt(5)\n"
      "s_nop 0\n"
      "v_mfma_f32_32x32x16_bf16 a[128:143], a[80:83], v[204:207], a[128:143]\n"
      "ds_read_b128 a[112:115], v73 offset:49408\n"
      "v_exp_f32 v43, v43\n"
      "v_add_f32 v16, v16, v42\n"
      "v_cvt_pk_bf16_f32 v228, v40, v41\n"
      "v_fma_f32 v45, v45, s54, -v14\n"
      "v_mfma_f32_32x32x16_bf16 v[96:111], a[84:87], a[28:31], v[96:111]\n"
      "ds_read_b64_tr_b16 a[116:117], v3 offset:8192\n"
      "ds_read_b64_tr_b16 a[118:119], v3 offset:9216\n"
      "v_exp_f32 v44, v44\n"
      "v_add_f32 v17, v17, v43\n"
      "v_fma_f32 v46, v46, s54, -v14\n"
      "v_mfma_f32_32x32x16_bf16 v[112:127], a[88:91], a[28:31], v[112:127]\n"
      "ds_read_b128 a[120:123], v74 offset:33024\n"
      "buffer_load_dwordx4 v7, s[44:47], s61 offen offset:1024 nt lds\n"
      "v_exp_f32 v45, v45\n"
      "v_add_f32 v16, v16, v44\n"
      "v_cvt_pk_bf16_f32 v229, v42, v43\n"
      "v_mfma_f32_32x32x16_bf16 a[144:159], a[92:95], v[204:207], a[144:159]\n"
      "ds_read_b128 a[124:127], v74 offset:49408\n"
      "v_fma_f32 v47, v47, s54, -v14\n"
      "v_exp_f32 v46, v46\n"
      "s_waitcnt lgkmcnt(5)\n"
      "v_mfma_f32_32x32x16_bf16 v[96:111], a[96:99], a[32:35], v[96:111]\n"
      "ds_read_b64_tr_b16 a[64:65], v2 offset:10240\n"
      "ds_read_b64_tr_b16 a[66:67], v2 offset:11264\n"
      "v_add_f32_e64 v17, v17, v45\n"
      "v_fma_f32 v48, v48, s54, -v14\n"
      "v_exp_f32_e64 v47, v47\n"
      "v_mfma_f32_32x32x16_bf16 v[112:127], a[100:103], a[32:35], v[112:127]\n"
      "ds_read_b128 a[68:71], v75 offset:33024\n"
      "buffer_load_dwordx4 v8, s[44:47], s61 offen offset:2048 nt lds\n"
      "v_add_f32_e64 v16, v16, v46\n"
      "v_cvt_pk_bf16_f32 v230, v44, v45\n"
      "v_fma_f32 v49, v49, s54, -v14\n"
      "v_mfma_f32_32x32x16_bf16 a[128:143], a[104:107], v[208:211], a[128:143]\n"
      "ds_read_b128 a[72:75], v75 offset:49408\n"
      "v_exp_f32 v48, v48\n"
      "v_add_f32 v17, v17, v47\n"
      "v_fma_f32 v50, v50, s54, -v14\n"
      "v_exp_f32_e64 v49, v49\n"
      "v_mfma_f32_32x32x16_bf16 v[96:111], a[108:111], a[36:39], v[96:111]\n"
      "ds_read_b64_tr_b16 a[76:77], v3 offset:10240\n"
      "ds_read_b64_tr_b16 a[78:79], v3 offset:11264\n"
      "v_add_f32 v16, v16, v48\n"
      "s_waitcnt lgkmcnt(6)\n"
      "v_cvt_pk_bf16_f32 v231, v46, v47\n"
      "v_mfma_f32_32x32x16_bf16 v[112:127], a[112:115], a[36:39], v[112:127]\n"
      "ds_read_b128 a[80:83], v76 offset:33024\n"
      "buffer_load_dwordx4 v9, s[44:47], s61 offen offset:3072 nt lds\n"
      "v_fma_f32 v51, v51, s54, -v14\n"
      "v_exp_f32_e64 v50, v50\n"
      "v_mfma_f32_32x32x16_bf16 a[144:159], a[116:119], v[208:211], a[144:159]\n"
      "ds_read_b128 a[84:87], v76 offset:49408\n"
      "v_add_f32_e64 v17, v17, v49\n"
      "v_fma_f32 v52, v52, s54, -v14\n"
      "v_exp_f32 v51, v51\n"
      "v_add_f32 v16, v16, v50\n"
      "v_mfma_f32_32x32x16_bf16 v[96:111], a[120:123], a[40:43], v[96:111]\n"
      "ds_read_b64_tr_b16 a[88:89], v2 offset:12288\n"
      "ds_read_b64_tr_b16 a[90:91], v2 offset:13312\n"
      "v_cvt_pk_bf16_f32 v232, v48, v49\n"
      "v_fma_f32 v53, v53, s54, -v14\n"
      "v_exp_f32_e64 v52, v52\n"
      "v_mfma_f32_32x32x16_bf16 v[112:127], a[124:127], a[40:43], v[112:127]\n"
      "ds_read_b128 a[92:95], v77 offset:33024\n"
      "v_add_f32_e64 v17, v17, v51\n"
      "v_fma_f32 v54, v54, s54, -v14\n"
      "v_exp_f32 v53, v53\n"
      "s_waitcnt lgkmcnt(5)\n"
      "v_mfma_f32_32x32x16_bf16 a[128:143], a[64:67], v[212:215], a[128:143]\n"
      "ds_read_b128 a[96:99], v77 offset:49408\n"
      "s_add_u32 m0, s58, 81920\n"
      "s_mov_b32 s61, 65792\n"
      "v_add_f32_e64 v16, v16, v52\n"
      "v_cvt_pk_bf16_f32 v233, v50, v51\n"
      "v_mfma_f32_32x32x16_bf16 v[96:111], a[68:71], a[44:47], v[96:111]\n"
      "ds_read_b64_tr_b16 a[100:101], v3 offset:12288\n"
      "ds_read_b64_tr_b16 a[102:103], v3 offset:13312\n"
      "buffer_load_dwordx4 v6, s[44:47], s61 offen offset:0 nt lds\n"
      "v_fma_f32 v55, v55, s54, -v14\n"
      "v_exp_f32_e64 v54, v54\n"
      "v_mfma_f32_32x32x16_bf16 v[112:127], a[72:75], a[44:47], v[112:127]\n"
      "ds_read_b128 a[104:107], v78 offset:33024\n"
      "v_add_f32_e64 v17, v17, v53\n"
      "v_fma_f32 v56, v56, s54, -v14\n"
      "v_exp_f32_e64 v55, v55\n"
      "v_mfma_f32_32x32x16_bf16 a[144:159], a[76:79], v[212:215], a[144:159]\n"
      "ds_read_b128 a[108:111], v78 offset:49408\n"
      "v_add_f32_e64 v16, v16, v54\n"
      "v_cvt_pk_bf16_f32 v234, v52, v53\n"
      "v_fma_f32 v57, v57, s54, -v14\n"
      "v_exp_f32 v56, v56\n"
      "s_waitcnt lgkmcnt(5)\n"
      "v_mfma_f32_32x32x16_bf16 v[96:111], a[80:83], a[48:51], v[96:111]\n"
      "ds_read_b64_tr_b16 a[112:113], v2 offset:14336\n"
      "ds_read_b64_tr_b16 a[114:115], v2 offset:15360\n"
      "buffer_load_dwordx4 v7, s[44:47], s61 offen offset:1024 nt lds\n"
      "v_add_f32_e64 v17, v17, v55\n"
      "v_mfma_f32_32x32x16_bf16 v[112:127], a[84:87], a[48:51], v[112:127]\n"
      "ds_read_b128 a[116:119], v79 offset:33024\n"
      "v_fma_f32 v58, v58, s54, -v14\n"
      "v_exp_f32 v57, v57\n"
      "v_add_f32 v16, v16, v56\n"
      "v_cvt_pk_bf16_f32 v235, v54, v55\n"
      "v_mfma_f32_32x32x16_bf16 a[128:143], a[88:91], v[216:219], a[128:143]\n"
      "ds_read_b128 a[120:123], v79 offset:49408\n"
      "v_fma_f32 v59, v59, s54, -v14\n"
      "v_exp_f32 v58, v58\n"
      "v_add_f32 v17, v17, v57\n"
      "v_fma_f32 v60, v60, s54, -v14\n"
      "v_mfma_f32_32x32x16_bf16 v[96:111], a[92:95], a[52:55], v[96:111]\n"
      "ds_read_b64_tr_b16 a[124:125], v3 offset:14336\n"
      "ds_read_b64_tr_b16 a[126:127], v3 offset:15360\n"
      "buffer_load_dwordx4 v8, s[44:47], s61 offen offset:2048 nt lds\n"
      "s_waitcnt lgkmcnt(6)\n"
      "s_nop 0\n"
      "v_mfma_f32_32x32x16_bf16 v[112:127], a[96:99], a[52:55], v[112:127]\n"
      "v_exp_f32 v59, v59\n"
      "v_add_f32 v16, v16, v58\n"
      "v_cvt_pk_bf16_f32 v236, v56, v57\n"
      "v_fma_f32 v61, v61, s54, -v14\n"
      "v_exp_f32_e64 v60, v60\n"
      "v_mfma_f32_32x32x16_bf16 a[144:159], a[100:103], v[216:219], a[144:159]\n"
      "v_add_f32_e64 v17, v17, v59\n"
      "v_fma_f32 v62, v62, s54, -v14\n"
      "v_exp_f32 v61, v61\n"
      "v_add_f32 v16, v16, v60\n"
      "v_mfma_f32_32x32x16_bf16 v[96:111], a[104:107], a[56:59], v[96:111]\n"
      "buffer_load_dwordx4 v9, s[44:47], s61 offen offset:3072 nt lds\n"
      "v_cvt_pk_bf16_f32 v237, v58, v59\n"
      "v_fma_f32 v63, v63, s54, -v14\n"
      "v_exp_f32_e64 v62, v62\n"
      "v_mfma_f32_32x32x16_bf16 v[112:127], a[108:111], a[56:59], v[112:127]\n"
      "s_waitcnt lgkmcnt(0)\n"
      "s_waitcnt vmcnt(20)\n"
      "s_barrier\n"
      "s_nop 0\n"
      "v_mfma_f32_32x32x16_bf16 a[128:143], a[112:115], v[220:223], a[128:143]\n"
      "ds_read_b64_tr_b16 a[64:65], v2 offset:16384\n"
      "ds_read_b64_tr_b16 a[66:67], v2 offset:17408\n"
      "ds_read_b64_tr_b16 a[68:69], v3 offset:16384\n"
      "ds_read_b64_tr_b16 a[70:71], v3 offset:17408\n"
      "ds_read_b64_tr_b16 a[72:73], v2 offset:18432\n"
      "ds_read_b64_tr_b16 a[74:75], v2 offset:19456\n"
      "v_mfma_f32_32x32x16_bf16 v[96:111], a[116:119], a[60:63], v[96:111]\n"
      "ds_read_b64_tr_b16 a[76:77], v3 offset:18432\n"
      "ds_read_b64_tr_b16 a[78:79], v3 offset:19456\n"
      "v_add_f32 v17, v17, v61\n"
      "v_exp_f32 v63, v63\n"
      "v_add_f32_e64 v16, v16, v62\n"
      "v_mfma_f32_32x32x16_bf16 v[112:127], a[120:123], a[60:63], v[112:127]\n"
      "ds_read_b64_tr_b16 a[80:81], v2 offset:20480\n"
      "ds_read_b64_tr_b16 a[82:83], v2 offset:21504\n"
      "s_add_u32 m0, s58, 98304\n"
      "s_mov_b32 s61, 131328\n"
      "v_cvt_pk_bf16_f32 v238, v60, v61\n"
      "v_add_f32_e64 v17, v17, v63\n"
      "v_mfma_f32_32x32x16_bf16 a[144:159], a[124:127], v[220:223], a[144:159]\n"
      "ds_read_b64_tr_b16 a[84:85], v3 offset:20480\n"
      "ds_read_b64_tr_b16 a[86:87], v3 offset:21504\n"
      "buffer_load_dwordx4 v6, s[44:47], s61 offen offset:0 nt lds\n"
      "v_cvt_pk_bf16_f32 v239, v62, v63\n"
      "s_waitcnt lgkmcnt(8)\n"
      "s_nop 0\n"
      "v_mfma_f32_32x32x16_bf16 v[64:79], a[64:67], v[128:131], 0\n"
      "ds_read_b64_tr_b16 a[88:89], v2 offset:22528\n"
      "ds_read_b64_tr_b16 a[90:91], v2 offset:23552\n"
      "v_mfma_f32_32x32x16_bf16 v[80:95], a[68:71], v[128:131], 0\n"
      "ds_read_b64_tr_b16 a[92:93], v3 offset:22528\n"
      "ds_read_b64_tr_b16 a[94:95], v3 offset:23552\n"
      "s_waitcnt lgkmcnt(8)\n"
      "s_nop 0\n"
      "v_mfma_f32_32x32x16_bf16 v[64:79], a[72:75], v[132:135], v[64:79]\n"
      "ds_read_b64_tr_b16 a[96:97], v2 offset:24576\n"
      "ds_read_b64_tr_b16 a[98:99], v2 offset:25600\n"
      "buffer_load_dwordx4 v7, s[44:47], s61 offen offset:1024 nt lds\n"
      "v_max3_f32 v18, v96, v97, v98\n"
      "v_max3_f32 v19, v111, v112, v113\n"
      "v_mfma_f32_32x32x16_bf16 v[80:95], a[76:79], v[132:135], v[80:95]\n"
      "ds_read_b64_tr_b16 a[100:101], v3 offset:24576\n"
      "ds_read_b64_tr_b16 a[102:103], v3 offset:25600\n"
      "v_max3_f32 v18, v18, v99, v100\n"
      "v_max3_f32 v19, v19, v114, v115\n"
      "v_max3_f32 v18, v18, v101, v102\n"
      "v_max3_f32 v19, v19, v116, v117\n"
      "s_waitcnt lgkmcnt(8)\n"
      "s_nop 0\n"
      "v_mfma_f32_32x32x16_bf16 v[64:79], a[80:83], v[136:139], v[64:79]\n"
      "ds_read_b64_tr_b16 a[104:105], v2 offset:26624\n"
      "ds_read_b64_tr_b16 a[106:107], v2 offset:27648\n"
      "v_max3_f32 v18, v18, v103, v104\n"
      "v_max3_f32 v19, v19, v118, v119\n"
      "v_max3_f32 v18, v18, v105, v106\n"
      "v_max3_f32 v19, v19, v120, v121\n"
      "v_mfma_f32_32x32x16_bf16 v[80:95], a[84:87], v[136:139], v[80:95]\n"
      "ds_read_b64_tr_b16 a[108:109], v3 offset:26624\n"
      "ds_read_b64_tr_b16 a[110:111], v3 offset:27648\n"
      "buffer_load_dwordx4 v8, s[44:47], s61 offen offset:2048 nt lds\n"
      "v_max3_f32 v18, v18, v107, v108\n"
      "s_waitcnt lgkmcnt(8)\n"
      "s_nop 0\n"
      "v_mfma_f32_32x32x16_bf16 v[64:79], a[88:91], v[140:143], v[64:79]\n"
      "ds_read_b64_tr_b16 a[112:113], v2 offset:28672\n"
      "ds_read_b64_tr_b16 a[114:115], v2 offset:29696\n"
      "v_max3_f32 v19, v19, v122, v123\n"
      "v_max3_f32 v18, v18, v109, v110\n"
      "v_max3_f32 v19, v19, v124, v125\n"
      "v_max3_f32 v19, v19, v126, v127\n"
      "v_max_f32_e64 v18, v18, v19\n"
      "v_mfma_f32_32x32x16_bf16 v[80:95], a[92:95], v[140:143], v[80:95]\n"
      "ds_read_b64_tr_b16 a[116:117], v3 offset:28672\n"
      "ds_read_b64_tr_b16 a[118:119], v3 offset:29696\n"
      "v_cmp_gt_f32 vcc, v18, v15\n"
      "s_cbranch_vccnz .Lrare10_%=\n"
      ".Lret10_%=:\n"
      "v_fma_f32 v96, v96, s54, -v14\n"
      "s_waitcnt lgkmcnt(8)\n"
      "s_nop 0\n"
      "v_mfma_f32_32x32x16_bf16 v[64:79], a[96:99], v[144:147], v[64:79]\n"
      "ds_read_b64_tr_b16 a[120:121], v2 offset:30720\n"
      "ds_read_b64_tr_b16 a[122:123], v2 offset:31744\n"
      "buffer_load_dwordx4 v9, s[44:47], s61 offen offset:3072 nt lds\n"
      "v_fma_f32 v97, v97, s54, -v14\n"
      "v_exp_f32_e64 v96, v96\n"
      "v_mfma_f32_32x32x16_bf16 v[80:95], a[100:103], v[144:147], v[80:95]\n"
      "ds_read_b64_tr_b16 a[124:125], v3 offset:30720\n"
      "ds_read_b64_tr_b16 a[126:127], v3 offset:31744\n"
      "v_fma_f32 v98, v98, s54, -v14\n"
      "v_exp_f32 v97, v97\n"
      "s_waitcnt lgkmcnt(8)\n"
      "v_mfma_f32_32x32x16_bf16 v[64:79], a[104:107], v[148:151], v[64:79]\n"
      "ds_read_b64_tr_b16 a[64:65], v10 offset:0\n"
      "ds_read_b64_tr_b16 a[66:67], v10 offset:1024\n"
      "v_add_f32_e64 v16, v16, v96\n"
      "v_fma_f32 v99, v99, s54, -v14\n"
      "v_exp_f32_e64 v98, v98\n"
      "v_mfma_f32_32x32x16_bf16 v[80:95], a[108:111], v[148:151], v[80:95]\n"
      ".Lpad3_%=:\n"
      "s_cbranch_execz .LpadretA_%=\n"
      "s_nop 0\n"
      "ds_read_b64_tr_b16 a[68:69], v11 offset:0\n"
      "ds_read_b64_tr_b16 a[70:71], v11 offset:1024\n"
      "v_add_f32_e64 v17, v17, v97\n"
      "v_fma_f32 v100, v100, s54, -v14\n"
      "v_exp_f32 v99, v99\n"
      "s_waitcnt lgkmcnt(8)\n"
      "v_mfma_f32_32x32x16_bf16 v[64:79], a[112:115], v[152:155], v[64:79]\n"
      "ds_read_b64_tr_b16 a[72:73], v10 offset:2048\n"
      "ds_read_b64_tr_b16 a[74:75], v10 offset:3072\n"
      "s_add_u32 m0, s58, 114688\n"
      "s_mov_b32 s61, 196864\n"
      "v_add_f32_e64 v16, v16, v98\n"
      "v_cvt_pk_bf16_f32 v240, v96, v97\n"
      "v_mfma_f32_32x32x16_bf16 v[80:95], a[116:119], v[152:155], v[80:95]\n"
      "ds_read_b64_tr_b16 a[76:77], v11 offset:2048\n"
      "ds_read_b64_tr_b16 a[78:79], v11 offset:3072\n"
      "buffer_load_dwordx4 v6, s[44:47], s61 offen offset:0 nt lds\n"
      "v_fma_f32 v101, v101, s54, -v14\n"
      "s_waitcnt lgkmcnt(8)\n"
      "s_nop 0\n"
      "v_mfma_f32_32x32x16_bf16 v[64:79], a[120:123], v[156:159], v[64:79]\n"
      "ds_read_b64_tr_b16 a[80:81], v10 offset:4096\n"
      "ds_read_b64_tr_b16 a[82:83], v10 offset:5120\n"
      "v_exp_f32 v100, v100\n"
      "v_add_f32 v17, v17, v99\n"
      "v_fma_f32 v102, v102, s54, -v14\n"
      "v_mfma_f32_32x32x16_bf16 v[80:95], a[124:127], v[156:159], v[80:95]\n"
      "ds_read_b64_tr_b16 a[84:85], v11 offset:4096\n"
      "ds_read_b64_tr_b16 a[86:87], v11 offset:5120\n"
      "v_exp_f32 v101, v101\n"
      "v_add_f32 v16, v16, v100\n"
      "v_cvt_pk_bf16_f32 v241, v98, v99\n"
      "s_waitcnt lgkmcnt(8)\n"
      "s_nop 0\n"
      "v_mfma_f32_32x32x16_bf16 v[64:79], a[64:67], v[160:163], v[64:79]\n"
      "ds_read_b64_tr_b16 a[88:89], v10 offset:6144\n"
      "ds_read_b64_tr_b16 a[90:91], v10 offset:7168\n"
      "buffer_load_dwordx4 v7, s[44:47], s61 offen offset:1024 nt lds\n"
      "v_fma_f32 v103, v103, s54, -v14\n"
      "v_exp_f32_e64 v102, v102\n"
      "v_mfma_f32_32x32x16_bf16 v[80:95], a[68:71], v[160:163], v[80:95]\n"
      "ds_read_b64_tr_b16 a[92:93], v11 offset:6144\n"
      "ds_read_b64_tr_b16 a[94:95], v11 offset:7168\n"
      "v_add_f32 v17, v17, v101\n"
      "s_waitcnt lgkmcnt(8)\n"
      "v_fma_f32 v104, v104, s54, -v14\n"
      "v_mfma_f32_32x32x16_bf16 v[64:79], a[72:75], v[164:167], v[64:79]\n"
      "ds_read_b64_tr_b16 a[96:97], v10 offset:8192\n"
      "ds_read_b64_tr_b16 a[98:99], v10 offset:9216\n"
      "v_exp_f32 v103, v103\n"
      "v_add_f32 v16, v16, v102\n"
      "v_cvt_pk_bf16_f32 v242, v100, v101\n"
      "v_fma_f32 v105, v105, s54, -v14\n"
      "v_mfma_f32_32x32x16_bf16 v[80:95], a[76:79], v[164:167], v[80:95]\n"
      "ds_read_b64_tr_b16 a[100:101], v11 offset:8192\n"
      "ds_read_b64_tr_b16 a[102:103], v11 offset:9216\n"
      "buffer_load_dwordx4 v8, s[44:47], s61 offen offset:2048 nt lds\n"
      "v_exp_f32 v104, v104\n"
      "s_waitcnt lgkmcnt(8)\n"
      "v_mfma_f32_32x32x16_bf16 v[64:79], a[80:83], v[168:171], v[64:79]\n"
      "ds_read_b64_tr_b16 a[104:105], v10 offset:10240\n"
      "ds_read_b64_tr_b16 a[106:107], v10 offset:11264\n"
      "v_add_f32_e64 v17, v17, v103\n"
      "v_fma_f32 v106, v106, s54, -v14\n"
      "v_exp_f32_e64 v105, v105\n"
      "v_mfma_f32_32x32x16_bf16 v[80:95], a[84:87], v[168:171], v[80:95]\n"
      "ds_read_b64_tr_b16 a[108:109], v11 offset:10240\n"
      "ds_read_b64_tr_b16 a[110:111], v11 offset:11264\n"
      "v_add_f32 v16, v16, v104\n"
      "s_waitcnt lgkmcnt(8)\n"
      "v_cvt_pk_bf16_f32 v243, v102, v103\n"
      "v_fma_f32 v107, v107, s54, -v14\n"
      "v_mfma_f32_32x32x16_bf16 v[64:79], a[88:91], v[172:175], v[64:79]\n"
      "ds_read_b64_tr_b16 a[112:113], v10 offset:12288\n"
      "ds_read_b64_tr_b16 a[114:115], v10 offset:13312\n"
      "buffer_load_dwordx4 v9, s[44:47], s61 offen offset:3072 nt lds\n"
      "v_exp_f32 v106, v106\n"
      "v_add_f32 v17, v17, v105\n"
      "v_mfma_f32_32x32x16_bf16 v[80:95], a[92:95], v[172:175], v[80:95]\n"
      "ds_read_b64_tr_b16 a[116:117], v11 offset:12288\n"
      "ds_read_b64_tr_b16 a[118:119], v11 offset:13312\n"
      "v_fma_f32 v108, v108, s54, -v14\n"
      "v_exp_f32 v107, v107\n"
      "s_waitcnt lgkmcnt(8)\n"
      "v_mfma_f32_32x32x16_bf16 v[64:79], a[96:99], v[176:179], v[64:79]\n"
      "ds_read_b64_tr_b16 a[120:121], v10 offset:14336\n"
      "ds_read_b64_tr_b16 a[122:123], v10 offset:15360\n"
      "v_add_f32_e64 v16, v16, v106\n"
      "v_cvt_pk_bf16_f32 v244, v104, v105\n"
      "v_fma_f32 v109, v109, s54, -v14\n"
      "v_exp_f32_e64 v108, v108\n"
      "v_mfma_f32_32x32x16_bf16 v[80:95], a[100:103], v[176:179], v[80:95]\n"
      "ds_read_b64_tr_b16 a[124:125], v11 offset:14336\n"
      "ds_read_b64_tr_b16 a[126:127], v11 offset:15360\n"
      "s_waitcnt vmcnt(20)\n"
      "s_barrier\n"
      "s_waitcnt lgkmcnt(8)\n"
      "s_nop 0\n"
      "v_mfma_f32_32x32x16_bf16 v[64:79], a[104:107], v[180:183], v[64:79]\n"
      "ds_read_b64_tr_b16 a[64:65], v10 offset:16384\n"
      "ds_read_b64_tr_b16 a[66:67], v10 offset:17408\n"
      "s_add_u32 m0, s58, 131072\n"
      "s_mov_b32 s61, 384\n"
      "v_add_f32_e64 v17, v17, v107\n"
      "v_fma_f32 v110, v110, s54, -v14\n"
      "v_mfma_f32_32x32x16_bf16 v[80:95], a[108:111], v[180:183], v[80:95]\n"
      "ds_read_b64_tr_b16 a[68:69], v11 offset:16384\n"
      "ds_read_b64_tr_b16 a[70:71], v11 offset:17408\n"
      "buffer_load_dwordx4 v6, s[44:47], s61 offen offset:0 nt lds\n"
      "v_exp_f32 v109, v109\n"
      "s_waitcnt lgkmcnt(8)\n"
      "v_mfma_f32_32x32x16_bf16 v[64:79], a[112:115], v[184:187], v[64:79]\n"
      "ds_read_b64_tr_b16 a[72:73], v10 offset:18432\n"
      "ds_read_b64_tr_b16 a[74:75], v10 offset:19456\n"
      "v_add_f32_e64 v16, v16, v108\n"
      "v_cvt_pk_bf16_f32 v245, v106, v107\n"
      "v_fma_f32 v111, v111, s54, -v14\n"
      "v_mfma_f32_32x32x16_bf16 v[80:95], a[116:119], v[184:187], v[80:95]\n"
      "ds_read_b64_tr_b16 a[76:77], v11 offset:18432\n"
      "ds_read_b64_tr_b16 a[78:79], v11 offset:19456\n"
      "v_exp_f32 v110, v110\n"
      "v_add_f32 v17, v17, v109\n"
      "v_fma_f32 v112, v112, s54, -v14\n"
      "s_waitcnt lgkmcnt(8)\n"
      "s_nop 0\n"
      "v_mfma_f32_32x32x16_bf16 v[64:79], a[120:123], v[188:191], v[64:79]\n"
      "ds_read_b64_tr_b16 a[80:81], v10 offset:20480\n"
      "ds_read_b64_tr_b16 a[82:83], v10 offset:21504\n"
      "buffer_load_dwordx4 v7, s[44:47], s61 offen offset:1024 nt lds\n"
      "v_exp_f32_e64 v111, v111\n"
      "v_mfma_f32_32x32x16_bf16 v[80:95], a[124:127], v[188:191], v[80:95]\n"
      "ds_read_b64_tr_b16 a[84:85], v11 offset:20480\n"
      "ds_read_b64_tr_b16 a[86:87], v11 offset:21504\n"
      "v_add_f32 v16, v16, v110\n"
      "s_waitcnt lgkmcnt(8)\n"
      "v_cvt_pk_bf16_f32 v246, v108, v109\n"
      "v_fma_f32 v113, v113, s54, -v14\n"
      "v_mfma_f32_32x32x16_bf16 v[64:79], a[64:67], v[192:195], v[64:79]\n"
      "ds_read_b64_tr_b16 a[88:89], v10 offset:22528\n"
      "ds_read_b64_tr_b16 a[90:91], v10 offset:23552\n"
      "v_exp_f32 v112, v112\n"
      "v_add_f32 v17, v17, v111\n"
      "v_fma_f32 v114, v114, s54, -v14\n"
      "v_exp_f32_e64 v113, v113\n"
      "v_mfma_f32_32x32x16_bf16 v[80:95], a[68:71], v[192:195], v[80:95]\n"
      "ds_read_b64_tr_b16 a[92:93], v11 offset:22528\n"
      "ds_read_b64_tr_b16 a[94:95], v11 offset:23552\n"
      "buffer_load_dwordx4 v8, s[44:47], s61 offen offset:2048 nt lds\n"
      "v_add_f32 v16, v16, v112\n"
      "s_waitcnt lgkmcnt(8)\n"
      "v_mfma_f32_32x32x16_bf16 v[64:79], a[72:75], v[196:199], v[64:79]\n"
      "ds_read_b64_tr_b16 a[96:97], v10 offset:24576\n"
      "ds_read_b64_tr_b16 a[98:99], v10 offset:25600\n"
      "v_cvt_pk_bf16_f32 v247, v110, v111\n"
      "v_fma_f32 v115, v115, s54, -v14\n"
      "v_exp_f32_e64 v114, v114\n"
      "v_mfma_f32_32x32x16_bf16 v[80:95], a[76:79], v[196:199], v[80:95]\n"
      "ds_read_b64_tr_b16 a[100:101], v11 offset:24576\n"
      "ds_read_b64_tr_b16 a[102:103], v11 offset:25600\n"
      "v_add_f32_e64 v17, v17, v113\n"
      "v_fma_f32 v116, v116, s54, -v14\n"
      "v_exp_f32 v115, v115\n"
      "s_waitcnt lgkmcnt(8)\n"
      "v_mfma_f32_32x32x16_bf16 v[64:79], a[80:83], v[200:203], v[64:79]\n"
      "ds_read_b64_tr_b16 a[104:105], v10 offset:26624\n"
      "ds_read_b64_tr_b16 a[106:107], v10 offset:27648\n"
      "buffer_load_dwordx4 v9, s[44:47], s61 offen offset:3072 nt lds\n"
      "v_add_f32_e64 v16, v16, v114\n"
      "v_cvt_pk_bf16_f32 v248, v112, v113\n"
      "v_mfma_f32_32x32x16_bf16 v[80:95], a[84:87], v[200:203], v[80:95]\n"
      "ds_read_b64_tr_b16 a[108:109], v11 offset:26624\n"
      "ds_read_b64_tr_b16 a[110:111], v11 offset:27648\n"
      "v_fma_f32 v117, v117, s54, -v14\n"
      "v_exp_f32 v116, v116\n"
      "s_waitcnt lgkmcnt(8)\n"
      "v_mfma_f32_32x32x16_bf16 v[64:79], a[88:91], v[204:207], v[64:79]\n"
      "ds_read_b64_tr_b16 a[112:113], v10 offset:28672\n"
      "ds_read_b64_tr_b16 a[114:115], v10 offset:29696\n"
      "v_add_f32_e64 v17, v17, v115\n"
      "v_fma_f32 v118, v118, s54, -v14\n"
      "v_exp_f32_e64 v117, v117\n"
      "v_mfma_f32_32x32x16_bf16 v[80:95], a[92:95], v[204:207], v[80:95]\n"
      "ds_read_b64_tr_b16 a[116:117], v11 offset:28672\n"
      "ds_read_b64_tr_b16 a[118:119], v11 offset:29696\n"
      "v_add_f32 v16, v16, v116\n"
      "s_waitcnt lgkmcnt(8)\n"
      "v_cvt_pk_bf16_f32 v249, v114, v115\n"
      "v_fma_f32 v119, v119, s54, -v14\n"
      "v_mfma_f32_32x32x16_bf16 v[64:79], a[96:99], v[208:211], v[64:79]\n"
      "ds_read_b64_tr_b16 a[120:121], v10 offset:30720\n"
      "ds_read_b64_tr_b16 a[122:123], v10 offset:31744\n"
      "s_add_u32 m0, s58, 147456\n"
      "s_mov_b32 s61, 65920\n"
      "v_exp_f32 v118, v118\n"
      "v_add_f32 v17, v17, v117\n"
      "v_mfma_f32_32x32x16_bf16 v[80:95], a[100:103], v[208:211], v[80:95]\n"
      "ds_read_b64_tr_b16 a[124:125], v11 offset:30720\n"
      "ds_read_b64_tr_b16 a[126:127], v11 offset:31744\n"
      "buffer_load_dwordx4 v6, s[44:47], s61 offen offset:0 nt lds\n"
      "v_fma_f32 v120, v120, s54, -v14\n"
      "s_waitcnt lgkmcnt(8)\n"
      "s_nop 0\n"
      "v_mfma_f32_32x32x16_bf16 v[64:79], a[104:107], v[212:215], v[64:79]\n"
      "ds_read_b64_tr_b16 a[64:65], v10 offset:32768\n"
      "ds_read_b64_tr_b16 a[66:67], v10 offset:33792\n"
      "v_exp_f32 v119, v119\n"
      "v_add_f32 v16, v16, v118\n"
      "v_cvt_pk_bf16_f32 v250, v116, v117\n"
      "v_fma_f32 v121, v121, s54, -v14\n"
      "v_mfma_f32_32x32x16_bf16 v[80:95], a[108:111], v[212:215], v[80:95]\n"
      "ds_read_b64_tr_b16 a[68:69], v11 offset:32768\n"
      "ds_read_b64_tr_b16 a[70:71], v11 offset:33792\n"
      "v_exp_f32_e64 v120, v120\n"
      "v_add_f32 v17, v17, v119\n"
      "s_waitcnt lgkmcnt(8)\n"
      "v_mfma_f32_32x32x16_bf16 v[64:79], a[112:115], v[216:219], v[64:79]\n"
      "ds_read_b64_tr_b16 a[72:73], v10 offset:34816\n"
      "ds_read_b64_tr_b16 a[74:75], v10 offset:35840\n"
      "buffer_load_dwordx4 v7, s[44:47], s61 offen offset:1024 nt lds\n"
      "v_fma_f32 v122, v122, s54, -v14\n"
      "v_exp_f32_e64 v121, v121\n"
      "v_mfma_f32_32x32x16_bf16 v[80:95], a[116:119], v[216:219], v[80:95]\n"
      "ds_read_b64_tr_b16 a[76:77], v11 offset:34816\n"
      "ds_read_b64_tr_b16 a[78:79], v11 offset:35840\n"
      "v_add_f32 v16, v16, v120\n"
      "s_waitcnt lgkmcnt(8)\n"
      "v_cvt_pk_bf16_f32 v251, v118, v119\n"
      "v_fma_f32 v123, v123, s54, -v14\n"
      "v_mfma_f32_32x32x16_bf16 v[64:79], a[120:123], v[220:223], v[64:79]\n"
      "ds_read_b64_tr_b16 a[80:81], v10 offset:36864\n"
      "ds_read_b64_tr_b16 a[82:83], v10 offset:37888\n"
      "v_exp_f32 v122, v122\n"
      "v_add_f32 v17, v17, v121\n"
      "v_fma_f32 v124, v124, s54, -v14\n"
      "v_mfma_f32_32x32x16_bf16 v[80:95], a[124:127], v[220:223], v[80:95]\n"
      "ds_read_b64_tr_b16 a[84:85], v11 offset:36864\n"
      "ds_read_b64_tr_b16 a[86:87], v11 offset:37888\n"
      "buffer_load_dwordx4 v8, s[44:47], s61 offen offset:2048 nt lds\n"
      "v_exp_f32 v123, v123\n"
      "s_waitcnt lgkmcnt(8)\n"
      "v_mfma_f32_32x32x16_bf16 a[128:143], a[64:67], v[224:227], a[128:143]\n"
      "ds_read_b64_tr_b16 a[88:89], v10 offset:38912\n"
      "ds_read_b64_tr_b16 a[90:91], v10 offset:39936\n"
      "v_add_f32_e64 v16, v16, v122\n"
      "v_cvt_pk_bf16_f32 v252, v120, v121\n"
      "v_fma_f32 v125, v125, s54, -v14\n"
      "v_exp_f32_e64 v124, v124\n"
      "v_mfma_f32_32x32x16_bf16 a[144:159], a[68:71], v[224:227], a[144:159]\n"
      "ds_read_b64_tr_b16 a[92:93], v11 offset:38912\n"
      "ds_read_b64_tr_b16 a[94:95], v11 offset:39936\n"
      "v_add_f32 v17, v17, v123\n"
      "s_waitcnt lgkmcnt(8)\n"
      "v_fma_f32 v126, v126, s54, -v14\n"
      "v_mfma_f32_32x32x16_bf16 a[128:143], a[72:75], v[228:231], a[128:143]\n"
      "ds_read_b64_tr_b16 a[96:97], v10 offset:40960\n"
      "ds_read_b64_tr_b16 a[98:99], v10 offset:41984\n"
      "buffer_load_dwordx4 v9, s[44:47], s61 offen offset:3072 nt lds\n"
      "v_exp_f32 v125, v125\n"
      "v_add_f32 v16, v16, v124\n"
      "v_mfma_f32_32x32x16_bf16 a[144:159], a[76:79], v[228:231], a[144:159]\n"
      "ds_read_b64_tr_b16 a[100:101], v11 offset:40960\n"
      "ds_read_b64_tr_b16 a[102:103], v11 offset:41984\n"
      "v_cvt_pk_bf16_f32 v253, v122, v123\n"
      "v_fma_f32 v127, v127, s54, -v14\n"
      "v_exp_f32 v126, v126\n"
      "s_waitcnt lgkmcnt(8)\n"
      "v_mfma_f32_32x32x16_bf16 a[128:143], a[80:83], v[232:235], a[128:143]\n"
      "ds_read_b64_tr_b16 a[104:105], v10 offset:43008\n"
      "ds_read_b64_tr_b16 a[106:107], v10 offset:44032\n"
      "v_add_f32 v17, v17, v125\n"
      "v_exp_f32 v127, v127\n"
      "v_add_f32_e64 v16, v16, v126\n"
      "v_mfma_f32_32x32x16_bf16 a[144:159], a[84:87], v[232:235], a[144:159]\n"
      "ds_read_b64_tr_b16 a[108:109], v11 offset:43008\n"
      "ds_read_b64_tr_b16 a[110:111], v11 offset:44032\n"
      "v_cvt_pk_bf16_f32 v254, v124, v125\n"
      "v_add_f32 v17, v17, v127\n"
      "s_waitcnt lgkmcnt(8)\n"
      "v_cvt_pk_bf16_f32 v255, v126, v127\n"
      "v_mfma_f32_32x32x16_bf16 a[128:143], a[88:91], v[236:239], a[128:143]\n"
      "ds_read_b64_tr_b16 a[112:113], v10 offset:45056\n"
      "ds_read_b64_tr_b16 a[114:115], v10 offset:46080\n"
      "v_add_f32_e64 v16, v16, v17\n"
      "v_mov_b32 v17, v16\n"
      "s_nop 1\n"
      "v_mfma_f32_32x32x16_bf16 a[144:159], a[92:95], v[236:239], a[144:159]\n"
      "ds_read_b64_tr_b16 a[116:117], v11 offset:45056\n"
      "ds_read_b64_tr_b16 a[118:119], v11 offset:46080\n"
      "v_permlane32_swap_b32 v17, v16\n"
      "s_waitcnt lgkmcnt(8)\n"
      "v_mfma_f32_32x32x16_bf16 a[128:143], a[96:99], v[240:243], a[128:143]\n"
      "ds_read_b64_tr_b16 a[120:121], v10 offset:47104\n"
      "ds_read_b64_tr_b16 a[122:123], v10 offset:48128\n"
      "v_add_f32 v16, v16, v17\n"
      "v_rcp_f32 v20, v16\n"
      "v_mfma_f32_32x32x16_bf16 a[144:159], a[100:103], v[240:243], a[144:159]\n"
      "ds_read_b64_tr_b16 a[124:125], v11 offset:47104\n"
      "ds_read_b64_tr_b16 a[126:127], v11 offset:48128\n"
      "s_waitcnt vmcnt(20)\n"
      "s_barrier\n"
      "s_waitcnt lgkmcnt(8)\n"
      "s_nop 0\n"
      "v_mfma_f32_32x32x16_bf16 a[128:143], a[104:107], v[244:247], a[128:143]\n"
      "ds_read_b64_tr_b16 a[64:65], v10 offset:49152\n"
      "ds_read_b64_tr_b16 a[66:67], v10 offset:50176\n"
      "s_add_u32 m0, s58, 0\n"
      "s_nop 0\n"
      "s_mov_b32 s61, 131456\n"
      "buffer_load_dwordx4 v6, s[44:47], s61 offen offset:0 nt lds\n"
      "v_mfma_f32_32x32x16_bf16 a[144:159], a[108:111], v[244:247], a[144:159]\n"
      "ds_read_b64_tr_b16 a[68:69], v11 offset:49152\n"
      "ds_read_b64_tr_b16 a[70:71], v11 offset:50176\n"
      "s_waitcnt lgkmcnt(8)\n"
      "s_nop 0\n"
      "v_mfma_f32_32x32x16_bf16 a[128:143], a[112:115], v[248:251], a[128:143]\n"
      "ds_read_b64_tr_b16 a[72:73], v10 offset:51200\n"
      "ds_read_b64_tr_b16 a[74:75], v10 offset:52224\n"
      "v_mfma_f32_32x32x16_bf16 a[144:159], a[116:119], v[248:251], a[144:159]\n"
      "ds_read_b64_tr_b16 a[76:77], v11 offset:51200\n"
      "ds_read_b64_tr_b16 a[78:79], v11 offset:52224\n"
      "buffer_load_dwordx4 v7, s[44:47], s61 offen offset:1024 nt lds\n"
      "s_waitcnt lgkmcnt(8)\n"
      "s_nop 0\n"
      "v_mfma_f32_32x32x16_bf16 a[128:143], a[120:123], v[252:255], a[128:143]\n"
      "ds_read_b64_tr_b16 a[80:81], v10 offset:53248\n"
      "ds_read_b64_tr_b16 a[82:83], v10 offset:54272\n"
      "v_mfma_f32_32x32x16_bf16 a[144:159], a[124:127], v[252:255], a[144:159]\n"
      "ds_read_b64_tr_b16 a[84:85], v11 offset:53248\n"
      "ds_read_b64_tr_b16 a[86:87], v11 offset:54272\n"
      "s_waitcnt lgkmcnt(8)\n"
      "s_nop 0\n"
      "v_mfma_f32_32x32x16_bf16 v[64:79], a[64:67], v[224:227], v[64:79]\n"
      "ds_read_b64_tr_b16 a[88:89], v10 offset:55296\n"
      "ds_read_b64_tr_b16 a[90:91], v10 offset:56320\n"
      "buffer_load_dwordx4 v8, s[44:47], s61 offen offset:2048 nt lds\n"
      "v_mfma_f32_32x32x16_bf16 v[80:95], a[68:71], v[224:227], v[80:95]\n"
      "ds_read_b64_tr_b16 a[92:93], v11 offset:55296\n"
      "ds_read_b64_tr_b16 a[94:95], v11 offset:56320\n"
      "s_waitcnt lgkmcnt(8)\n"
      "s_nop 0\n"
      "v_mfma_f32_32x32x16_bf16 v[64:79], a[72:75], v[228:231], v[64:79]\n"
      "ds_read_b64_tr_b16 a[96:97], v10 offset:57344\n"
      "ds_read_b64_tr_b16 a[98:99], v10 offset:58368\n"
      "v_mfma_f32_32x32x16_bf16 v[80:95], a[76:79], v[228:231], v[80:95]\n"
      "ds_read_b64_tr_b16 a[100:101], v11 offset:57344\n"
      "ds_read_b64_tr_b16 a[102:103], v11 offset:58368\n"
      "buffer_load_dwordx4 v9, s[44:47], s61 offen offset:3072 nt lds\n"
      "v_accvgpr_read_b32 v26, a128\n"
      "s_waitcnt lgkmcnt(8)\n"
      "s_nop 0\n"
      "v_mfma_f32_32x32x16_bf16 v[64:79], a[80:83], v[232:235], v[64:79]\n"
      "ds_read_b64_tr_b16 a[104:105], v10 offset:59392\n"
      "ds_read_b64_tr_b16 a[106:107], v10 offset:60416\n"
      "v_accvgpr_read_b32 v27, a129\n"
      "v_accvgpr_read_b32 v28, a130\n"
      "v_accvgpr_read_b32 v29, a131\n"
      "v_mul_f32 v26, v20, v26\n"
      "v_mul_f32 v27, v20, v27\n"
      "v_mfma_f32_32x32x16_bf16 v[80:95], a[84:87], v[232:235], v[80:95]\n"
      "ds_read_b64_tr_b16 a[108:109], v11 offset:59392\n"
      "ds_read_b64_tr_b16 a[110:111], v11 offset:60416\n"
      "v_mul_f32 v28, v20, v28\n"
      "v_mul_f32 v29, v20, v29\n"
      "v_cvt_pk_bf16_f32 v32, v26, v27\n"
      "s_waitcnt lgkmcnt(8)\n"
      "s_nop 0\n"
      "v_mfma_f32_32x32x16_bf16 v[64:79], a[88:91], v[236:239], v[64:79]\n"
      "ds_read_b64_tr_b16 a[112:113], v10 offset:61440\n"
      "ds_read_b64_tr_b16 a[114:115], v10 offset:62464\n"
      "v_cvt_pk_bf16_f32 v33, v28, v29\n"
      "v_accvgpr_read_b32 v26, a132\n"
      "v_accvgpr_read_b32 v27, a133\n"
      "v_accvgpr_read_b32 v28, a134\n"
      "v_accvgpr_read_b32 v29, a135\n"
      "v_mfma_f32_32x32x16_bf16 v[80:95], a[92:95], v[236:239], v[80:95]\n"
      "ds_read_b64_tr_b16 a[116:117], v11 offset:61440\n"
      "ds_read_b64_tr_b16 a[118:119], v11 offset:62464\n"
      "s_add_u32 m0, s58, 16384\n"
      "s_mov_b32 s61, 196992\n"
      "v_mul_f32 v26, v20, v26\n"
      "s_waitcnt lgkmcnt(8)\n"
      "v_mfma_f32_32x32x16_bf16 v[64:79], a[96:99], v[240:243], v[64:79]\n"
      "ds_read_b64_tr_b16 a[120:121], v10 offset:63488\n"
      "ds_read_b64_tr_b16 a[122:123], v10 offset:64512\n"
      "buffer_load_dwordx4 v6, s[44:47], s61 offen offset:0 nt lds\n"
      "v_mul_f32 v27, v20, v27\n"
      "v_mul_f32 v28, v20, v28\n"
      "v_mul_f32_e64 v29, v20, v29\n"
      "v_mfma_f32_32x32x16_bf16 v[80:95], a[100:103], v[240:243], v[80:95]\n"
      "ds_read_b64_tr_b16 a[124:125], v11 offset:63488\n"
      "ds_read_b64_tr_b16 a[126:127], v11 offset:64512\n"
      "v_cvt_pk_bf16_f32 v34, v26, v27\n"
      "v_cvt_pk_bf16_f32 v35, v28, v29\n"
      "v_accvgpr_read_b32 v26, a136\n"
      "s_waitcnt lgkmcnt(8)\n"
      "s_nop 0\n"
      "v_mfma_f32_32x32x16_bf16 v[64:79], a[104:107], v[244:247], v[64:79]\n"
      "ds_read_b64_tr_b16 a[64:65], v12 offset:0\n"
      "ds_read_b64_tr_b16 a[66:67], v12 offset:1024\n"
      "v_accvgpr_read_b32 v27, a137\n"
      "v_accvgpr_read_b32 v28, a138\n"
      "v_accvgpr_read_b32 v29, a139\n"
      "v_mul_f32 v26, v20, v26\n"
      "v_mul_f32 v27, v20, v27\n"
      "v_mfma_f32_32x32x16_bf16 v[80:95], a[108:111], v[244:247], v[80:95]\n"
      "ds_read_b64_tr_b16 a[68:69], v13 offset:0\n"
      "ds_read_b64_tr_b16 a[70:71], v13 offset:1024\n"
      "buffer_load_dwordx4 v7, s[44:47], s61 offen offset:1024 nt lds\n"
      "v_mul_f32 v28, v20, v28\n"
      "s_waitcnt lgkmcnt(8)\n"
      "v_mfma_f32_32x32x16_bf16 v[64:79], a[112:115], v[248:251], v[64:79]\n"
      "ds_read_b64_tr_b16 a[72:73], v12 offset:2048\n"
      "ds_read_b64_tr_b16 a[74:75], v12 offset:3072\n"
      "v_mul_f32_e64 v29, v20, v29\n"
      "v_cvt_pk_bf16_f32 v36, v26, v27\n"
      "v_cvt_pk_bf16_f32 v37, v28, v29\n"
      "v_accvgpr_read_b32 v26, a140\n"
      "v_accvgpr_read_b32 v27, a141\n"
      "v_mfma_f32_32x32x16_bf16 v[80:95], a[116:119], v[248:251], v[80:95]\n"
      "ds_read_b64_tr_b16 a[76:77], v13 offset:2048\n"
      "ds_read_b64_tr_b16 a[78:79], v13 offset:3072\n"
      "v_accvgpr_read_b32 v28, a142\n"
      "v_accvgpr_read_b32 v29, a143\n"
      "v_mul_f32 v26, v20, v26\n"
      "s_waitcnt lgkmcnt(8)\n"
      "v_mfma_f32_32x32x16_bf16 v[64:79], a[120:123], v[252:255], v[64:79]\n"
      "ds_read_b64_tr_b16 a[80:81], v12 offset:4096\n"
      "ds_read_b64_tr_b16 a[82:83], v12 offset:5120\n"
      "buffer_load_dwordx4 v8, s[44:47], s61 offen offset:2048 nt lds\n"
      "v_mul_f32 v27, v20, v27\n"
      "v_mul_f32 v28, v20, v28\n"
      "v_mul_f32_e64 v29, v20, v29\n"
      "v_mfma_f32_32x32x16_bf16 v[80:95], a[124:127], v[252:255], v[80:95]\n"
      "ds_read_b64_tr_b16 a[84:85], v13 offset:4096\n"
      "ds_read_b64_tr_b16 a[86:87], v13 offset:5120\n"
      "v_cvt_pk_bf16_f32 v38, v26, v27\n"
      "v_cvt_pk_bf16_f32 v39, v28, v29\n"
      "v_accvgpr_read_b32 v26, a144\n"
      "s_waitcnt lgkmcnt(8)\n"
      "s_nop 0\n"
      "v_mfma_f32_32x32x16_bf16 v[96:111], a[64:67], v[128:131], 0\n"
      "ds_read_b64_tr_b16 a[88:89], v12 offset:6144\n"
      "ds_read_b64_tr_b16 a[90:91], v12 offset:7168\n"
      "v_accvgpr_read_b32 v27, a145\n"
      "v_accvgpr_read_b32 v28, a146\n"
      "v_accvgpr_read_b32 v29, a147\n"
      "v_mul_f32 v26, v20, v26\n"
      "v_mul_f32 v27, v20, v27\n"
      "v_mfma_f32_32x32x16_bf16 v[112:127], a[68:71], v[128:131], 0\n"
      "ds_read_b64_tr_b16 a[92:93], v13 offset:6144\n"
      "ds_read_b64_tr_b16 a[94:95], v13 offset:7168\n"
      "buffer_load_dwordx4 v9, s[44:47], s61 offen offset:3072 nt lds\n"
      "v_mul_f32 v28, v20, v28\n"
      "s_waitcnt lgkmcnt(8)\n"
      "v_mfma_f32_32x32x16_bf16 v[96:111], a[72:75], v[132:135], v[96:111]\n"
      "ds_read_b64_tr_b16 a[96:97], v12 offset:8192\n"
      "ds_read_b64_tr_b16 a[98:99], v12 offset:9216\n"
      "v_mul_f32_e64 v29, v20, v29\n"
      "v_cvt_pk_bf16_f32 v40, v26, v27\n"
      "v_cvt_pk_bf16_f32 v41, v28, v29\n"
      "v_accvgpr_read_b32 v26, a148\n"
      "v_accvgpr_read_b32 v27, a149\n"
      "v_mfma_f32_32x32x16_bf16 v[112:127], a[76:79], v[132:135], v[112:127]\n"
      "ds_read_b64_tr_b16 a[100:101], v13 offset:8192\n"
      "ds_read_b64_tr_b16 a[102:103], v13 offset:9216\n"
      "v_accvgpr_read_b32 v28, a150\n"
      "v_accvgpr_read_b32 v29, a151\n"
      "v_mul_f32 v26, v20, v26\n"
      "s_waitcnt lgkmcnt(8)\n"
      "v_mfma_f32_32x32x16_bf16 v[96:111], a[80:83], v[136:139], v[96:111]\n"
      "ds_read_b64_tr_b16 a[104:105], v12 offset:10240\n"
      "ds_read_b64_tr_b16 a[106:107], v12 offset:11264\n"
      "v_mul_f32 v27, v20, v27\n"
      "v_mul_f32 v28, v20, v28\n"
      "v_mul_f32_e64 v29, v20, v29\n"
      "v_cvt_pk_bf16_f32 v42, v26, v27\n"
      "v_cvt_pk_bf16_f32 v43, v28, v29\n"
      "v_mfma_f32_32x32x16_bf16 v[112:127], a[84:87], v[136:139], v[112:127]\n"
      "ds_read_b64_tr_b16 a[108:109], v13 offset:10240\n"
      "ds_read_b64_tr_b16 a[110:111], v13 offset:11264\n"
      "v_accvgpr_read_b32 v26, a152\n"
      "v_accvgpr_read_b32 v27, a153\n"
      "v_accvgpr_read_b32 v28, a154\n"
      "s_waitcnt lgkmcnt(8)\n"
      "s_nop 0\n"
      "v_mfma_f32_32x32x16_bf16 v[96:111], a[88:91], v[140:143], v[96:111]\n"
      "ds_read_b64_tr_b16 a[112:113], v12 offset:12288\n"
      "ds_read_b64_tr_b16 a[114:115], v12 offset:13312\n"
      "v_accvgpr_read_b32 v29, a155\n"
      "v_mul_f32 v26, v20, v26\n"
      "v_mul_f32 v27, v20, v27\n"
      "v_mul_f32 v28, v20, v28\n"
      "v_mul_f32 v29, v20, v29\n"
      "v_mfma_f32_32x32x16_bf16 v[112:127], a[92:95], v[140:143], v[112:127]\n"
      "ds_read_b64_tr_b16 a[116:117], v13 offset:12288\n"
      "ds_read_b64_tr_b16 a[118:119], v13 offset:13312\n"
      "v_cvt_pk_bf16_f32 v44, v26, v27\n"
      "v_cvt_pk_bf16_f32 v45, v28, v29\n"
      "v_accvgpr_read_b32 v26, a156\n"
      "s_waitcnt lgkmcnt(8)\n"
      "s_nop 0\n"
      "v_mfma_f32_32x32x16_bf16 v[96:111], a[96:99], v[144:147], v[96:111]\n"
      "ds_read_b64_tr_b16 a[120:121], v12 offset:14336\n"
      "ds_read_b64_tr_b16 a[122:123], v12 offset:15360\n"
      "v_accvgpr_read_b32 v27, a157\n"
      "v_accvgpr_read_b32 v28, a158\n"
      "v_accvgpr_read_b32 v29, a159\n"
      "v_mul_f32 v26, v20, v26\n"
      "v_mul_f32 v27, v20, v27\n"
      "v_mfma_f32_32x32x16_bf16 v[112:127], a[100:103], v[144:147], v[112:127]\n"
      "ds_read_b64_tr_b16 a[124:125], v13 offset:14336\n"
      "ds_read_b64_tr_b16 a[126:127], v13 offset:15360\n"
      "s_waitcnt vmcnt(20)\n"
      "s_barrier\n"
      "s_waitcnt lgkmcnt(8)\n"
      "s_nop 0\n"
      "v_mfma_f32_32x32x16_bf16 v[96:111], a[104:107], v[148:151], v[96:111]\n"
      "ds_read_b64_tr_b16 a[64:65], v12 offset:16384\n"
      "ds_read_b64_tr_b16 a[66:67], v12 offset:17408\n"
      "v_mul_f32 v28, v20, v28\n"
      "v_mul_f32 v29, v20, v29\n"
      "v_cvt_pk_bf16_f32 v46, v26, v27\n"
      "v_cvt_pk_bf16_f32 v47, v28, v29\n"
      "v_mfma_f32_32x32x16_bf16 v[112:127], a[108:111], v[148:151], v[112:127]\n"
      "ds_read_b64_tr_b16 a[68:69], v13 offset:16384\n"
      "ds_read_b64_tr_b16 a[70:71], v13 offset:17408\n"
      "v_mul_f32_e64 v64, v20, v64\n"
      "v_mul_f32 v65, v20, v65\n"
      "s_waitcnt lgkmcnt(8)\n"
      "v_mfma_f32_32x32x16_bf16 v[96:111], a[112:115], v[152:155], v[96:111]\n"
      "ds_read_b64_tr_b16 a[72:73], v12 offset:18432\n"
      "ds_read_b64_tr_b16 a[74:75], v12 offset:19456\n"
      "v_mul_f32 v66, v20, v66\n"
      "v_mul_f32 v67, v20, v67\n"
      "v_cvt_pk_bf16_f32 v48, v64, v65\n"
      "v_mfma_f32_32x32x16_bf16 v[112:127], a[116:119], v[152:155], v[112:127]\n"
      "ds_read_b64_tr_b16 a[76:77], v13 offset:18432\n"
      "ds_read_b64_tr_b16 a[78:79], v13 offset:19456\n"
      "v_cvt_pk_bf16_f32 v49, v66, v67\n"
      "v_mul_f32 v68, v20, v68\n"
      "s_waitcnt lgkmcnt(8)\n"
      "v_mfma_f32_32x32x16_bf16 v[96:111], a[120:123], v[156:159], v[96:111]\n"
      "ds_read_b64_tr_b16 a[80:81], v12 offset:20480\n"
      "ds_read_b64_tr_b16 a[82:83], v12 offset:21504\n"
      "v_mul_f32 v69, v20, v69\n"
      "v_mul_f32 v70, v20, v70\n"
      "v_mul_f32_e64 v71, v20, v71\n"
      "v_mfma_f32_32x32x16_bf16 v[112:127], a[124:127], v[156:159], v[112:127]\n"
      "ds_read_b64_tr_b16 a[84:85], v13 offset:20480\n"
      "ds_read_b64_tr_b16 a[86:87], v13 offset:21504\n"
      "v_cvt_pk_bf16_f32 v50, v68, v69\n"
      "v_cvt_pk_bf16_f32 v51, v70, v71\n"
      "s_waitcnt lgkmcnt(8)\n"
      "s_nop 0\n"
      "v_mfma_f32_32x32x16_bf16 v[96:111], a[64:67], v[160:163], v[96:111]\n"
      "ds_read_b64_tr_b16 a[88:89], v12 offset:22528\n"
      "ds_read_b64_tr_b16 a[90:91], v12 offset:23552\n"
      "v_mul_f32 v72, v20, v72\n"
      "v_mul_f32 v73, v20, v73\n"
      "v_mul_f32_e64 v74, v20, v74\n"
      "v_mfma_f32_32x32x16_bf16 v[112:127], a[68:71], v[160:163], v[112:127]\n"
      "ds_read_b64_tr_b16 a[92:93], v13 offset:22528\n"
      "ds_read_b64_tr_b16 a[94:95], v13 offset:23552\n"
      "v_mul_f32 v75, v20, v75\n"
      "s_waitcnt lgkmcnt(8)\n"
      "v_cvt_pk_bf16_f32 v52, v72, v73\n"
      "v_mfma_f32_32x32x16_bf16 v[96:111], a[72:75], v[164:167], v[96:111]\n"
      "ds_read_b64_tr_b16 a[96:97], v12 offset:24576\n"
      "ds_read_b64_tr_b16 a[98:99], v12 offset:25600\n"
      "v_cvt_pk_bf16_f32 v53, v74, v75\n"
      "v_mul_f32 v76, v20, v76\n"
      "v_mul_f32 v77, v20, v77\n"
      "v_mfma_f32_32x32x16_bf16 v[112:127], a[76:79], v[164:167], v[112:127]\n"
      "ds_read_b64_tr_b16 a[100:101], v13 offset:24576\n"
      "ds_read_b64_tr_b16 a[102:103], v13 offset:25600\n"
      "v_mul_f32_e64 v78, v20, v78\n"
      "v_mul_f32 v79, v20, v79\n"
      "s_waitcnt lgkmcnt(8)\n"
      "v_mfma_f32_32x32x16_bf16 v[96:111], a[80:83], v[168:171], v[96:111]\n"
      "ds_read_b64_tr_b16 a[104:105], v12 offset:26624\n"
      "ds_read_b64_tr_b16 a[106:107], v12 offset:27648\n"
      "v_cvt_pk_bf16_f32 v54, v76, v77\n"
      "v_cvt_pk_bf16_f32 v55, v78, v79\n"
      "v_mul_f32 v80, v20, v80\n"
      "v_mul_f32 v81, v20, v81\n"
      "v_mfma_f32_32x32x16_bf16 v[112:127], a[84:87], v[168:171], v[112:127]\n"
      "ds_read_b64_tr_b16 a[108:109], v13 offset:26624\n"
      "ds_read_b64_tr_b16 a[110:111], v13 offset:27648\n"
      "v_mul_f32_e64 v82, v20, v82\n"
      ".Lpad4_%=:\n"
      "s_cbranch_execz .LpadretB_%=\n"
      "s_nop 0\n"
      "v_mul_f32 v83, v20, v83\n"
      "s_waitcnt lgkmcnt(8)\n"
      "v_mfma_f32_32x32x16_bf16 v[96:111], a[88:91], v[172:175], v[96:111]\n"
      "ds_read_b64_tr_b16 a[112:113], v12 offset:28672\n"
      "ds_read_b64_tr_b16 a[114:115], v12 offset:29696\n"
      "v_cvt_pk_bf16_f32 v56, v80, v81\n"
      "v_cvt_pk_bf16_f32 v57, v82, v83\n"
      "v_mul_f32_e64 v84, v20, v84\n"
      "v_mfma_f32_32x32x16_bf16 v[112:127], a[92:95], v[172:175], v[112:127]\n"
      "ds_read_b64_tr_b16 a[116:117], v13 offset:28672\n"
      "ds_read_b64_tr_b16 a[118:119], v13 offset:29696\n"
      "v_mul_f32_e64 v85, v20, v85\n"
      "v_mul_f32 v86, v20, v86\n"
      "s_waitcnt lgkmcnt(8)\n"
      "v_mfma_f32_32x32x16_bf16 v[96:111], a[96:99], v[176:179], v[96:111]\n"
      "ds_read_b64_tr_b16 a[120:121], v12 offset:30720\n"
      "ds_read_b64_tr_b16 a[122:123], v12 offset:31744\n"
      "v_mul_f32_e64 v87, v20, v87\n"
      "v_cvt_pk_bf16_f32 v58, v84, v85\n"
      "v_cvt_pk_bf16_f32 v59, v86, v87\n"
      "v_mfma_f32_32x32x16_bf16 v[112:127], a[100:103], v[176:179], v[112:127]\n"
      "ds_read_b64_tr_b16 a[124:125], v13 offset:30720\n"
      "ds_read_b64_tr_b16 a[126:127], v13 offset:31744\n"
      "v_mul_f32_e64 v88, v20, v88\n"
      "v_mul_f32 v89, v20, v89\n"
      "s_waitcnt lgkmcnt(8)\n"
      "v_mfma_f32_32x32x16_bf16 v[96:111], a[104:107], v[180:183], v[96:111]\n"
      "ds_read_b64_tr_b16 a[64:65], v12 offset:32768\n"
      "ds_read_b64_tr_b16 a[66:67], v12 offset:33792\n"
      "v_mul_f32 v90, v20, v90\n"
      "v_mul_f32 v91, v20, v91\n"
      "v_cvt_pk_bf16_f32 v60, v88, v89\n"
      "v_mfma_f32_32x32x16_bf16 v[112:127], a[108:111], v[180:183], v[112:127]\n"
      "ds_read_b64_tr_b16 a[68:69], v13 offset:32768\n"
      "ds_read_b64_tr_b16 a[70:71], v13 offset:33792\n"
      "v_cvt_pk_bf16_f32 v61, v90, v91\n"
      "v_mul_f32 v92, v20, v92\n"
      "s_waitcnt lgkmcnt(8)\n"
      "v_mfma_f32_32x32x16_bf16 v[96:111], a[112:115], v[184:187], v[96:111]\n"
      "ds_read_b64_tr_b16 a[72:73], v12 offset:34816\n"
      "ds_read_b64_tr_b16 a[74:75], v12 offset:35840\n"
      "ds_write_b64 v21, v[32:33] offset:0\n"
      "v_mfma_f32_32x32x16_bf16 v[112:127], a[116:119], v[184:187], v[112:127]\n"
      "ds_read_b64_tr_b16 a[76:77], v13 offset:34816\n"
      "ds_read_b64_tr_b16 a[78:79], v13 offset:35840\n"
      "v_mul_f32_e64 v93, v20, v93\n"
      "v_mul_f32 v94, v20, v94\n"
      "s_waitcnt lgkmcnt(9)\n"
      "v_mfma_f32_32x32x16_bf16 v[96:111], a[120:123], v[188:191], v[96:111]\n"
      "ds_read_b64_tr_b16 a[80:81], v12 offset:36864\n"
      "ds_read_b64_tr_b16 a[82:83], v12 offset:37888\n"
      "ds_write_b64 v21, v[34:35] offset:16\n"
      "v_mfma_f32_32x32x16_bf16 v[112:127], a[124:127], v[188:191], v[112:127]\n"
      "ds_read_b64_tr_b16 a[84:85], v13 offset:36864\n"
      "ds_read_b64_tr_b16 a[86:87], v13 offset:37888\n"
      "ds_write_b64 v21, v[36:37] offset:32\n"
      "s_waitcnt lgkmcnt(11)\n"
      "s_nop 0\n"
      "v_mfma_f32_32x32x16_bf16 v[96:111], a[64:67], v[192:195], v[96:111]\n"
      "ds_read_b64_tr_b16 a[88:89], v12 offset:38912\n"
      "ds_read_b64_tr_b16 a[90:91], v12 offset:39936\n"
      "v_mul_f32_e64 v95, v20, v95\n"
      "v_cvt_pk_bf16_f32 v62, v92, v93\n"
      "v_cvt_pk_bf16_f32 v63, v94, v95\n"
      "v_mfma_f32_32x32x16_bf16 v[112:127], a[68:71], v[192:195], v[112:127]\n"
      "ds_read_b64_tr_b16 a[92:93], v13 offset:38912\n"
      "ds_read_b64_tr_b16 a[94:95], v13 offset:39936\n"
      "ds_write_b64 v21, v[38:39] offset:48\n"
      "s_waitcnt lgkmcnt(11)\n"
      "s_nop 0\n"
      "v_mfma_f32_32x32x16_bf16 v[96:111], a[72:75], v[196:199], v[96:111]\n"
      "ds_read_b64_tr_b16 a[96:97], v12 offset:40960\n"
      "ds_read_b64_tr_b16 a[98:99], v12 offset:41984\n"
      "ds_write_b64 v21, v[40:41] offset:64\n"
      "v_mfma_f32_32x32x16_bf16 v[112:127], a[76:79], v[196:199], v[112:127]\n"
      "ds_read_b64_tr_b16 a[100:101], v13 offset:40960\n"
      "ds_read_b64_tr_b16 a[102:103], v13 offset:41984\n"
      "s_waitcnt lgkmcnt(11)\n"
      "s_nop 0\n"
      "v_mfma_f32_32x32x16_bf16 v[96:111], a[80:83], v[200:203], v[96:111]\n"
      "ds_read_b64_tr_b16 a[104:105], v12 offset:43008\n"
      "ds_read_b64_tr_b16 a[106:107], v12 offset:44032\n"
      "ds_write_b64 v21, v[42:43] offset:80\n"
      "v_mfma_f32_32x32x16_bf16 v[112:127], a[84:87], v[200:203], v[112:127]\n"
      "ds_read_b64_tr_b16 a[108:109], v13 offset:43008\n"
      "ds_read_b64_tr_b16 a[110:111], v13 offset:44032\n"
      "ds_write_b64 v21, v[44:45] offset:96\n"
      "s_waitcnt lgkmcnt(12)\n"
      "s_nop 0\n"
      "v_mfma_f32_32x32x16_bf16 v[96:111], a[88:91], v[204:207], v[96:111]\n"
      "ds_read_b64_tr_b16 a[112:113], v12 offset:45056\n"
      "ds_read_b64_tr_b16 a[114:115], v12 offset:46080\n"
      "v_mfma_f32_32x32x16_bf16 v[112:127], a[92:95], v[204:207], v[112:127]\n"
      "ds_read_b64_tr_b16 a[116:117], v13 offset:45056\n"
      "ds_read_b64_tr_b16 a[118:119], v13 offset:46080\n"
      "ds_write_b64 v21, v[46:47] offset:112\n"
      "s_waitcnt lgkmcnt(11)\n"
      "s_nop 0\n"
      "v_mfma_f32_32x32x16_bf16 v[96:111], a[96:99], v[208:211], v[96:111]\n"
      "ds_read_b64_tr_b16 a[120:121], v12 offset:47104\n"
      "ds_read_b64_tr_b16 a[122:123], v12 offset:48128\n"
      "ds_read_b64 a[0:1], v22 offset:0\n"
      "ds_read_b64 a[2:3], v24 offset:0\n"
      "ds_read_b64 a[4:5], v22 offset:288\n"
      "v_mfma_f32_32x32x16_bf16 v[112:127], a[100:103], v[208:211], v[112:127]\n"
      "ds_read_b64_tr_b16 a[124:125], v13 offset:47104\n"
      "ds_read_b64_tr_b16 a[126:127], v13 offset:48128\n"
      "s_waitcnt vmcnt(12)\n"
      "s_barrier\n"
      "s_waitcnt lgkmcnt(13)\n"
      "s_nop 0\n"
      "v_mfma_f32_32x32x16_bf16 v[96:111], a[104:107], v[212:215], v[96:111]\n"
      "ds_read_b64_tr_b16 a[64:65], v12 offset:49152\n"
      "ds_read_b64_tr_b16 a[66:67], v12 offset:50176\n"
      "ds_read_b64 a[6:7], v24 offset:288\n"
      "ds_read_b64 a[8:9], v22 offset:576\n"
      "ds_read_b64 a[10:11], v24 offset:576\n"
      "v_mfma_f32_32x32x16_bf16 v[112:127], a[108:111], v[212:215], v[112:127]\n"
      "ds_read_b64_tr_b16 a[68:69], v13 offset:49152\n"
      "ds_read_b64_tr_b16 a[70:71], v13 offset:50176\n"
      "ds_read_b64 a[12:13], v22 offset:864\n"
      "ds_read_b64 a[14:15], v24 offset:864\n"
      "s_waitcnt lgkmcnt(15)\n"
      "s_nop 0\n"
      "v_mfma_f32_32x32x16_bf16 v[96:111], a[112:115], v[216:219], v[96:111]\n"
      "ds_read_b64_tr_b16 a[72:73], v12 offset:51200\n"
      "ds_read_b64_tr_b16 a[74:75], v12 offset:52224\n"
      "ds_write_b64 v21, v[48:49] offset:0\n"
      "v_mfma_f32_32x32x16_bf16 v[112:127], a[116:119], v[216:219], v[112:127]\n"
      "ds_read_b64_tr_b16 a[76:77], v13 offset:51200\n"
      "ds_read_b64_tr_b16 a[78:79], v13 offset:52224\n"
      "s_waitcnt lgkmcnt(14)\n"
      "s_nop 0\n"
      "v_mfma_f32_32x32x16_bf16 v[96:111], a[120:123], v[220:223], v[96:111]\n"
      "ds_read_b64_tr_b16 a[80:81], v12 offset:53248\n"
      "ds_read_b64_tr_b16 a[82:83], v12 offset:54272\n"
      "ds_write_b64 v21, v[50:51] offset:16\n"
      "v_mfma_f32_32x32x16_bf16 v[112:127], a[124:127], v[220:223], v[112:127]\n"
      "ds_read_b64_tr_b16 a[84:85], v13 offset:53248\n"
      "ds_read_b64_tr_b16 a[86:87], v13 offset:54272\n"
      "ds_write_b64 v21, v[52:53] offset:32\n"
      "s_waitcnt lgkmcnt(13)\n"
      "s_nop 0\n"
      "v_mfma_f32_32x32x16_bf16 v[96:111], a[64:67], v[224:227], v[96:111]\n"
      "ds_read_b64_tr_b16 a[88:89], v12 offset:55296\n"
      "ds_read_b64_tr_b16 a[90:91], v12 offset:56320\n"
      "v_mfma_f32_32x32x16_bf16 v[112:127], a[68:71], v[224:227], v[112:127]\n"
      "ds_read_b64_tr_b16 a[92:93], v13 offset:55296\n"
      "ds_read_b64_tr_b16 a[94:95], v13 offset:56320\n"
      "ds_write_b64 v21, v[54:55] offset:48\n"
      "s_waitcnt lgkmcnt(11)\n"
      "s_nop 0\n"
      "v_mfma_f32_32x32x16_bf16 v[96:111], a[72:75], v[228:231], v[96:111]\n"
      "ds_read_b64_tr_b16 a[96:97], v12 offset:57344\n"
      "ds_read_b64_tr_b16 a[98:99], v12 offset:58368\n"
      "ds_write_b64 v21, v[56:57] offset:64\n"
      "v_mfma_f32_32x32x16_bf16 v[112:127], a[76:79], v[228:231], v[112:127]\n"
      "ds_read_b64_tr_b16 a[100:101], v13 offset:57344\n"
      "ds_read_b64_tr_b16 a[102:103], v13 offset:58368\n"
      "s_waitcnt lgkmcnt(11)\n"
      "s_nop 0\n"
      "v_mfma_f32_32x32x16_bf16 v[96:111], a[80:83], v[232:235], v[96:111]\n"
      "ds_read_b64_tr_b16 a[104:105], v12 offset:59392\n"
      "ds_read_b64_tr_b16 a[106:107], v12 offset:60416\n"
      "ds_write_b64 v21, v[58:59] offset:80\n"
      "v_mfma_f32_32x32x16_bf16 v[112:127], a[84:87], v[232:235], v[112:127]\n"
      "ds_read_b64_tr_b16 a[108:109], v13 offset:59392\n"
      "ds_read_b64_tr_b16 a[110:111], v13 offset:60416\n"
      "ds_write_b64 v21, v[60:61] offset:96\n"
      "s_waitcnt lgkmcnt(12)\n"
      "s_nop 0\n"
      "v_mfma_f32_32x32x16_bf16 v[96:111], a[88:91], v[236:239], v[96:111]\n"
      "ds_read_b64_tr_b16 a[112:113], v12 offset:61440\n"
      "ds_read_b64_tr_b16 a[114:115], v12 offset:62464\n"
      "v_mfma_f32_32x32x16_bf16 v[112:127], a[92:95], v[236:239], v[112:127]\n"
      "ds_read_b64_tr_b16 a[116:117], v13 offset:61440\n"
      "ds_read_b64_tr_b16 a[118:119], v13 offset:62464\n"
      "ds_write_b64 v21, v[62:63] offset:112\n"
      "s_waitcnt lgkmcnt(11)\n"
      "s_nop 0\n"
      "v_mfma_f32_32x32x16_bf16 v[96:111], a[96:99], v[240:243], v[96:111]\n"
      "ds_read_b64_tr_b16 a[120:121], v12 offset:63488\n"
      "ds_read_b64_tr_b16 a[122:123], v12 offset:64512\n"
      "buffer_store_dwordx4 a[0:3], v23, s[48:51], 0 offen offset:0 sc1\n"
      "ds_read_b64 a[16:17], v22 offset:0\n"
      "v_mfma_f32_32x32x16_bf16 v[112:127], a[100:103], v[240:243], v[112:127]\n"
      "ds_read_b64_tr_b16 a[124:125], v13 offset:63488\n"
      "ds_read_b64_tr_b16 a[126:127], v13 offset:64512\n"
      "ds_read_b64 a[18:19], v24 offset:0\n"
      "ds_read_b64 a[20:21], v22 offset:288\n"
      "s_waitcnt lgkmcnt(13)\n"
      "s_nop 0\n"
      "v_mfma_f32_32x32x16_bf16 v[96:111], a[104:107], v[244:247], v[96:111]\n"
      "ds_read_b64_tr_b16 a[64:65], v2 offset:0\n"
      "ds_read_b64_tr_b16 a[66:67], v2 offset:1024\n"
      "ds_read_b64 a[22:23], v24 offset:288\n"
      "ds_read_b64 a[24:25], v22 offset:576\n"
      "ds_read_b64 a[26:27], v24 offset:576\n"
      "v_mfma_f32_32x32x16_bf16 v[112:127], a[108:111], v[244:247], v[112:127]\n"
      "ds_read_b64_tr_b16 a[68:69], v3 offset:0\n"
      "ds_read_b64_tr_b16 a[70:71], v3 offset:1024\n"
      "ds_read_b64 a[28:29], v22 offset:864\n"
      "ds_read_b64 a[30:31], v24 offset:864\n"
      "s_waitcnt lgkmcnt(15)\n"
      "s_nop 0\n"
      "v_mfma_f32_32x32x16_bf16 v[96:111], a[112:115], v[248:251], v[96:111]\n"
      "ds_read_b64_tr_b16 a[72:73], v2 offset:2048\n"
      "ds_read_b64_tr_b16 a[74:75], v2 offset:3072\n"
      "v_mfma_f32_32x32x16_bf16 v[112:127], a[116:119], v[248:251], v[112:127]\n"
      "ds_read_b64_tr_b16 a[76:77], v3 offset:2048\n"
      "ds_read_b64_tr_b16 a[78:79], v3 offset:3072\n"
      "buffer_store_dwordx4 a[4:7], v23, s[48:51], 0 offen offset:1024 sc1\n"
      "s_waitcnt lgkmcnt(15)\n"
      "s_nop 0\n"
      "v_mfma_f32_32x32x16_bf16 v[96:111], a[120:123], v[252:255], v[96:111]\n"
      "ds_read_b64_tr_b16 a[80:81], v2 offset:4096\n"
      "ds_read_b64_tr_b16 a[82:83], v2 offset:5120\n"
      "v_mfma_f32_32x32x16_bf16 v[112:127], a[124:127], v[252:255], v[112:127]\n"
      "ds_read_b64_tr_b16 a[84:85], v3 offset:4096\n"
      "ds_read_b64_tr_b16 a[86:87], v3 offset:5120\n"
      "s_waitcnt lgkmcnt(10)\n"
      "s_nop 0\n"
      "v_mfma_f32_32x32x16_bf16 v[64:79], a[64:67], v[128:131], 0\n"
      "ds_read_b64_tr_b16 a[88:89], v2 offset:6144\n"
      "ds_read_b64_tr_b16 a[90:91], v2 offset:7168\n"
      "v_mfma_f32_32x32x16_bf16 v[80:95], a[68:71], v[128:131], 0\n"
      "ds_read_b64_tr_b16 a[92:93], v3 offset:6144\n"
      "ds_read_b64_tr_b16 a[94:95], v3 offset:7168\n"
      "s_waitcnt lgkmcnt(8)\n"
      "s_nop 0\n"
      "v_mfma_f32_32x32x16_bf16 v[64:79], a[72:75], v[132:135], v[64:79]\n"
      "ds_read_b64_tr_b16 a[96:97], v2 offset:8192\n"
      "ds_read_b64_tr_b16 a[98:99], v2 offset:9216\n"
      "buffer_store_dwordx4 a[8:11], v23, s[48:51], 0 offen offset:2048 sc1\n"
      "v_mfma_f32_32x32x16_bf16 v[80:95], a[76:79], v[132:135], v[80:95]\n"
      "ds_read_b64_tr_b16 a[100:101], v3 offset:8192\n"
      "ds_read_b64_tr_b16 a[102:103], v3 offset:9216\n"
      "v_mul_f32 v96, v20, v96\n"
      "v_mul_f32 v97, v20, v97\n"
      "v_mul_f32 v98, v20, v98\n"
      "s_waitcnt lgkmcnt(8)\n"
      "v_mfma_f32_32x32x16_bf16 v[64:79], a[80:83], v[136:139], v[64:79]\n"
      "ds_read_b64_tr_b16 a[104:105], v2 offset:10240\n"
      "ds_read_b64_tr_b16 a[106:107], v2 offset:11264\n"
      "v_mul_f32_e64 v99, v20, v99\n"
      "v_cvt_pk_bf16_f32 v96, v96, v97\n"
      "v_cvt_pk_bf16_f32 v97, v98, v99\n"
      "v_mfma_f32_32x32x16_bf16 v[80:95], a[84:87], v[136:139], v[80:95]\n"
      "ds_read_b64_tr_b16 a[108:109], v3 offset:10240\n"
      "ds_read_b64_tr_b16 a[110:111], v3 offset:11264\n"
      "v_mul_f32 v100, v20, v100\n"
      "v_mul_f32 v101, v20, v101\n"
      "v_mul_f32 v102, v20, v102\n"
      "s_waitcnt lgkmcnt(8)\n"
      "v_mfma_f32_32x32x16_bf16 v[64:79], a[88:91], v[140:143], v[64:79]\n"
      "ds_read_b64_tr_b16 a[112:113], v2 offset:12288\n"
      "ds_read_b64_tr_b16 a[114:115], v2 offset:13312\n"
      "v_mul_f32_e64 v103, v20, v103\n"
      "v_cvt_pk_bf16_f32 v98, v100, v101\n"
      "v_cvt_pk_bf16_f32 v99, v102, v103\n"
      "v_mfma_f32_32x32x16_bf16 v[80:95], a[92:95], v[140:143], v[80:95]\n"
      "ds_read_b64_tr_b16 a[116:117], v3 offset:12288\n"
      "ds_read_b64_tr_b16 a[118:119], v3 offset:13312\n"
      "buffer_store_dwordx4 a[12:15], v23, s[48:51], 0 offen offset:3072 sc1\n"
      "s_waitcnt lgkmcnt(8)\n"
      "s_nop 0\n"
      "v_mfma_f32_32x32x16_bf16 v[64:79], a[96:99], v[144:147], v[64:79]\n"
      "ds_read_b64_tr_b16 a[120:121], v2 offset:14336\n"
      "ds_read_b64_tr_b16 a[122:123], v2 offset:15360\n"
      "v_mul_f32 v104, v20, v104\n"
      "v_mul_f32 v105, v20, v105\n"
      "v_mul_f32 v106, v20, v106\n"
      "v_mul_f32 v107, v20, v107\n"
      "v_mfma_f32_32x32x16_bf16 v[80:95], a[100:103], v[144:147], v[80:95]\n"
      "ds_read_b64_tr_b16 a[124:125], v3 offset:14336\n"
      "ds_read_b64_tr_b16 a[126:127], v3 offset:15360\n"
      "s_waitcnt vmcnt(12)\n"
      "s_barrier\n"
      "s_waitcnt lgkmcnt(8)\n"
      "s_nop 0\n"
      "v_mfma_f32_32x32x16_bf16 v[64:79], a[104:107], v[148:151], v[64:79]\n"
      "ds_read_b64_tr_b16 a[64:65], v2 offset:16384\n"
      "ds_read_b64_tr_b16 a[66:67], v2 offset:17408\n"
      "v_cvt_pk_bf16_f32 v100, v104, v105\n"
      "v_cvt_pk_bf16_f32 v101, v106, v107\n"
      "v_mul_f32 v108, v20, v108\n"
      "v_mul_f32 v109, v20, v109\n"
      "v_mfma_f32_32x32x16_bf16 v[80:95], a[108:111], v[148:151], v[80:95]\n"
      "ds_read_b64_tr_b16 a[68:69], v3 offset:16384\n"
      "ds_read_b64_tr_b16 a[70:71], v3 offset:17408\n"
      "v_mul_f32_e64 v110, v20, v110\n"
      "v_mul_f32 v111, v20, v111\n"
      "s_waitcnt lgkmcnt(8)\n"
      "v_mfma_f32_32x32x16_bf16 v[64:79], a[112:115], v[152:155], v[64:79]\n"
      "ds_read_b64_tr_b16 a[72:73], v2 offset:18432\n"
      "ds_read_b64_tr_b16 a[74:75], v2 offset:19456\n"
      "buffer_store_dwordx4 a[16:19], v23, s[48:51], 0 offen offset:128 sc1\n"
      "v_cvt_pk_bf16_f32 v102, v108, v109\n"
      "v_mfma_f32_32x32x16_bf16 v[80:95], a[116:119], v[152:155], v[80:95]\n"
      "ds_read_b64_tr_b16 a[76:77], v3 offset:18432\n"
      "ds_read_b64_tr_b16 a[78:79], v3 offset:19456\n"
      "v_cvt_pk_bf16_f32 v103, v110, v111\n"
      "v_mul_f32 v112, v20, v112\n"
      "s_waitcnt lgkmcnt(8)\n"
      "v_mfma_f32_32x32x16_bf16 v[64:79], a[120:123], v[156:159], v[64:79]\n"
      "ds_read_b64_tr_b16 a[80:81], v2 offset:20480\n"
      "ds_read_b64_tr_b16 a[82:83], v2 offset:21504\n"
      "v_mul_f32 v113, v20, v113\n"
      "v_mul_f32 v114, v20, v114\n"
      "v_mul_f32_e64 v115, v20, v115\n"
      "v_cvt_pk_bf16_f32 v104, v112, v113\n"
      "v_mfma_f32_32x32x16_bf16 v[80:95], a[124:127], v[156:159], v[80:95]\n"
      "ds_read_b64_tr_b16 a[84:85], v3 offset:20480\n"
      "ds_read_b64_tr_b16 a[86:87], v3 offset:21504\n"
      "v_cvt_pk_bf16_f32 v105, v114, v115\n"
      "v_mul_f32 v116, v20, v116\n"
      "s_waitcnt lgkmcnt(8)\n"
      "v_mfma_f32_32x32x16_bf16 v[64:79], a[64:67], v[160:163], v[64:79]\n"
      "ds_read_b64_tr_b16 a[88:89], v2 offset:22528\n"
      "ds_read_b64_tr_b16 a[90:91], v2 offset:23552\n"
      "v_mul_f32 v117, v20, v117\n"
      "v_mul_f32 v118, v20, v118\n"
      "v_mul_f32_e64 v119, v20, v119\n"
      "v_cvt_pk_bf16_f32 v106, v116, v117\n"
      "v_mfma_f32_32x32x16_bf16 v[80:95], a[68:71], v[160:163], v[80:95]\n"
      "ds_read_b64_tr_b16 a[92:93], v3 offset:22528\n"
      "ds_read_b64_tr_b16 a[94:95], v3 offset:23552\n"
      "buffer_store_dwordx4 a[20:23], v23, s[48:51], 0 offen offset:1152 sc1\n"
      "s_waitcnt lgkmcnt(8)\n"
      "s_nop 0\n"
      "v_mfma_f32_32x32x16_bf16 v[64:79], a[72:75], v[164:167], v[64:79]\n"
      "ds_read_b64_tr_b16 a[96:97], v2 offset:24576\n"
      "ds_read_b64_tr_b16 a[98:99], v2 offset:25600\n"
      "v_cvt_pk_bf16_f32 v107, v118, v119\n"
      "v_mul_f32 v120, v20, v120\n"
      "v_mul_f32 v121, v20, v121\n"
      "v_mul_f32_e64 v122, v20, v122\n"
      "v_mfma_f32_32x32x16_bf16 v[80:95], a[76:79], v[164:167], v[80:95]\n"
      "ds_read_b64_tr_b16 a[100:101], v3 offset:24576\n"
      "ds_read_b64_tr_b16 a[102:103], v3 offset:25600\n"
      "v_mul_f32 v123, v20, v123\n"
      "s_waitcnt lgkmcnt(8)\n"
      "v_cvt_pk_bf16_f32 v108, v120, v121\n"
      "v_mfma_f32_32x32x16_bf16 v[64:79], a[80:83], v[168:171], v[64:79]\n"
      "ds_read_b64_tr_b16 a[104:105], v2 offset:26624\n"
      "ds_read_b64_tr_b16 a[106:107], v2 offset:27648\n"
      "v_cvt_pk_bf16_f32 v109, v122, v123\n"
      "v_mul_f32 v124, v20, v124\n"
      "v_mul_f32 v125, v20, v125\n"
      "v_mul_f32_e64 v126, v20, v126\n"
      "v_mfma_f32_32x32x16_bf16 v[80:95], a[84:87], v[168:171], v[80:95]\n"
      "ds_read_b64_tr_b16 a[108:109], v3 offset:26624\n"
      "ds_read_b64_tr_b16 a[110:111], v3 offset:27648\n"
      "v_mul_f32 v127, v20, v127\n"
      "s_waitcnt lgkmcnt(8)\n"
      "v_cvt_pk_bf16_f32 v110, v124, v125\n"
      "v_mfma_f32_32x32x16_bf16 v[64:79], a[88:91], v[172:175], v[64:79]\n"
      "ds_read_b64_tr_b16 a[112:113], v2 offset:28672\n"
      "ds_read_b64_tr_b16 a[114:115], v2 offset:29696\n"
      "buffer_store_dwordx4 a[24:27], v23, s[48:51], 0 offen offset:2176 sc1\n"
      "v_cvt_pk_bf16_f32 v111, v126, v127\n"
      "v_mfma_f32_32x32x16_bf16 v[80:95], a[92:95], v[172:175], v[80:95]\n"
      "ds_read_b64_tr_b16 a[116:117], v3 offset:28672\n"
      "ds_read_b64_tr_b16 a[118:119], v3 offset:29696\n"
      "ds_write_b64 v21, v[96:97] offset:0\n"
      "s_waitcnt lgkmcnt(9)\n"
      "s_nop 0\n"
      "v_mfma_f32_32x32x16_bf16 v[64:79], a[96:99], v[176:179], v[64:79]\n"
      "ds_read_b64_tr_b16 a[120:121], v2 offset:30720\n"
      "ds_read_b64_tr_b16 a[122:123], v2 offset:31744\n"
      "v_mfma_f32_32x32x16_bf16 v[80:95], a[100:103], v[176:179], v[80:95]\n"
      "ds_read_b64_tr_b16 a[124:125], v3 offset:30720\n"
      "ds_read_b64_tr_b16 a[126:127], v3 offset:31744\n"
      "s_waitcnt vmcnt(11)\n"
      "s_barrier\n"
      "s_waitcnt lgkmcnt(9)\n"
      "s_nop 0\n"
      "v_mfma_f32_32x32x16_bf16 v[64:79], a[104:107], v[180:183], v[64:79]\n"
      "ds_read_b64_tr_b16 a[64:65], v10 offset:0\n"
      "ds_read_b64_tr_b16 a[66:67], v10 offset:1024\n"
      "ds_write_b64 v21, v[98:99] offset:16\n"
      "v_mfma_f32_32x32x16_bf16 v[80:95], a[108:111], v[180:183], v[80:95]\n"
      "ds_read_b64_tr_b16 a[68:69], v11 offset:0\n"
      "ds_read_b64_tr_b16 a[70:71], v11 offset:1024\n"
      "buffer_store_dwordx4 a[28:31], v23, s[48:51], 0 offen offset:3200 sc1\n"
      "s_waitcnt lgkmcnt(10)\n"
      "s_nop 0\n"
      "v_mfma_f32_32x32x16_bf16 v[64:79], a[112:115], v[184:187], v[64:79]\n"
      "ds_read_b64_tr_b16 a[72:73], v10 offset:2048\n"
      "ds_read_b64_tr_b16 a[74:75], v10 offset:3072\n"
      "ds_write_b64 v21, v[100:101] offset:32\n"
      "v_mfma_f32_32x32x16_bf16 v[80:95], a[116:119], v[184:187], v[80:95]\n"
      "ds_read_b64_tr_b16 a[76:77], v11 offset:2048\n"
      "ds_read_b64_tr_b16 a[78:79], v11 offset:3072\n"
      "ds_write_b64 v21, v[102:103] offset:48\n"
      "s_waitcnt lgkmcnt(11)\n"
      "s_nop 0\n"
      "v_mfma_f32_32x32x16_bf16 v[64:79], a[120:123], v[188:191], v[64:79]\n"
      "ds_read_b64_tr_b16 a[80:81], v10 offset:4096\n"
      "ds_read_b64_tr_b16 a[82:83], v10 offset:5120\n"
      "ds_write_b64 v21, v[104:105] offset:64\n"
      "v_mfma_f32_32x32x16_bf16 v[80:95], a[124:127], v[188:191], v[80:95]\n"
      "ds_read_b64_tr_b16 a[84:85], v11 offset:4096\n"
      "ds_read_b64_tr_b16 a[86:87], v11 offset:5120\n"
      "s_waitcnt lgkmcnt(11)\n"
      "s_nop 0\n"
      "v_mfma_f32_32x32x16_bf16 v[64:79], a[64:67], v[192:195], v[64:79]\n"
      "ds_read_b64_tr_b16 a[88:89], v10 offset:6144\n"
      "ds_read_b64_tr_b16 a[90:91], v10 offset:7168\n"
      "ds_write_b64 v21, v[106:107] offset:80\n"
      "v_mfma_f32_32x32x16_bf16 v[80:95], a[68:71], v[192:195], v[80:95]\n"
      "ds_read_b64_tr_b16 a[92:93], v11 offset:6144\n"
      "ds_read_b64_tr_b16 a[94:95], v11 offset:7168\n"
      "ds_write_b64 v21, v[108:109] offset:96\n"
      "s_waitcnt lgkmcnt(12)\n"
      "s_nop 0\n"
      "v_mfma_f32_32x32x16_bf16 v[64:79], a[72:75], v[196:199], v[64:79]\n"
      "ds_read_b64_tr_b16 a[96:97], v10 offset:8192\n"
      "ds_read_b64_tr_b16 a[98:99], v10 offset:9216\n"
      "v_mfma_f32_32x32x16_bf16 v[80:95], a[76:79], v[196:199], v[80:95]\n"
      "ds_read_b64_tr_b16 a[100:101], v11 offset:8192\n"
      "ds_read_b64_tr_b16 a[102:103], v11 offset:9216\n"
      "ds_write_b64 v21, v[110:111] offset:112\n"
      "s_waitcnt lgkmcnt(11)\n"
      "s_nop 0\n"
      "v_mfma_f32_32x32x16_bf16 v[64:79], a[80:83], v[200:203], v[64:79]\n"
      "ds_read_b64_tr_b16 a[104:105], v10 offset:10240\n"
      "ds_read_b64_tr_b16 a[106:107], v10 offset:11264\n"
      "ds_read_b64 a[32:33], v22 offset:0\n"
      "ds_read_b64 a[34:35], v24 offset:0\n"
      "ds_read_b64 a[36:37], v22 offset:288\n"
      "v_mfma_f32_32x32x16_bf16 v[80:95], a[84:87], v[200:203], v[80:95]\n"
      "ds_read_b64_tr_b16 a[108:109], v11 offset:10240\n"
      "ds_read_b64_tr_b16 a[110:111], v11 offset:11264\n"
      "ds_read_b64 a[38:39], v24 offset:288\n"
      "ds_read_b64 a[40:41], v22 offset:576\n"
      "s_waitcnt lgkmcnt(15)\n"
      "s_nop 0\n"
      "v_mfma_f32_32x32x16_bf16 v[64:79], a[88:91], v[204:207], v[64:79]\n"
      "ds_read_b64_tr_b16 a[112:113], v10 offset:12288\n"
      "ds_read_b64_tr_b16 a[114:115], v10 offset:13312\n"
      "ds_read_b64 a[42:43], v24 offset:576\n"
      "ds_read_b64 a[44:45], v22 offset:864\n"
      "ds_read_b64 a[46:47], v24 offset:864\n"
      "v_mfma_f32_32x32x16_bf16 v[80:95], a[92:95], v[204:207], v[80:95]\n"
      "ds_read_b64_tr_b16 a[116:117], v11 offset:12288\n"
      "ds_read_b64_tr_b16 a[118:119], v11 offset:13312\n"
      "s_waitcnt lgkmcnt(15)\n"
      "s_nop 0\n"
      "v_mfma_f32_32x32x16_bf16 v[64:79], a[96:99], v[208:211], v[64:79]\n"
      "ds_read_b64_tr_b16 a[120:121], v10 offset:14336\n"
      "ds_read_b64_tr_b16 a[122:123], v10 offset:15360\n"
      "s_waitcnt lgkmcnt(14)\n"
      "s_nop 0\n"
      "buffer_store_dwordx4 a[32:35], v23, s[48:51], 0 offen offset:256 sc1\n"
      "v_mfma_f32_32x32x16_bf16 v[80:95], a[100:103], v[208:211], v[80:95]\n"
      "ds_read_b64_tr_b16 a[124:125], v11 offset:14336\n"
      "ds_read_b64_tr_b16 a[126:127], v11 offset:15360\n"
      "v_mfma_f32_32x32x16_bf16 v[64:79], a[104:107], v[212:215], v[64:79]\n"
      "s_waitcnt lgkmcnt(9)\n"
      "s_nop 0\n"
      "v_mfma_f32_32x32x16_bf16 v[80:95], a[108:111], v[212:215], v[80:95]\n"
      "s_waitcnt vmcnt(9)\n"
      "s_barrier\n"
      "v_mfma_f32_32x32x16_bf16 v[64:79], a[112:115], v[216:219], v[64:79]\n"
      "ds_read_b64_tr_b16 a[64:65], v10 offset:16384\n"
      "ds_read_b64_tr_b16 a[66:67], v10 offset:17408\n"
      "s_waitcnt lgkmcnt(4)\n"
      "s_nop 0\n"
      "v_mfma_f32_32x32x16_bf16 v[80:95], a[116:119], v[216:219], v[80:95]\n"
      "ds_read_b64_tr_b16 a[68:69], v10 offset:18432\n"
      "ds_read_b64_tr_b16 a[70:71], v10 offset:19456\n"
      "buffer_store_dwordx4 a[36:39], v23, s[48:51], 0 offen offset:1280 sc1\n"
      "v_mfma_f32_32x32x16_bf16 v[64:79], a[120:123], v[220:223], v[64:79]\n"
      "ds_read_b64_tr_b16 a[72:73], v10 offset:20480\n"
      "ds_read_b64_tr_b16 a[74:75], v10 offset:21504\n"
      "s_waitcnt lgkmcnt(4)\n"
      "s_nop 0\n"
      "v_mfma_f32_32x32x16_bf16 v[80:95], a[124:127], v[220:223], v[80:95]\n"
      "ds_read_b64_tr_b16 a[76:77], v10 offset:22528\n"
      "ds_read_b64_tr_b16 a[78:79], v10 offset:23552\n"
      "v_mfma_f32_32x32x16_bf16 v[64:79], a[64:67], v[224:227], v[64:79]\n"
      "ds_read_b64_tr_b16 a[80:81], v10 offset:24576\n"
      "ds_read_b64_tr_b16 a[82:83], v10 offset:25600\n"
      "s_waitcnt lgkmcnt(4)\n"
      "s_nop 0\n"
      "v_mfma_f32_32x32x16_bf16 v[64:79], a[68:71], v[228:231], v[64:79]\n"
      "ds_read_b64_tr_b16 a[84:85], v10 offset:26624\n"
      "ds_read_b64_tr_b16 a[86:87], v10 offset:27648\n"
      "v_mfma_f32_32x32x16_bf16 v[64:79], a[72:75], v[232:235], v[64:79]\n"
      "ds_read_b64_tr_b16 a[88:89], v10 offset:28672\n"
      "ds_read_b64_tr_b16 a[90:91], v10 offset:29696\n"
      "buffer_store_dwordx4 a[40:43], v23, s[48:51], 0 offen offset:2304 sc1\n"
      "s_waitcnt lgkmcnt(4)\n"
      "s_nop 0\n"
      "v_mfma_f32_32x32x16_bf16 v[64:79], a[76:79], v[236:239], v[64:79]\n"
      "ds_read_b64_tr_b16 a[92:93], v10 offset:30720\n"
      "ds_read_b64_tr_b16 a[94:95], v10 offset:31744\n"
      "v_mfma_f32_32x32x16_bf16 v[64:79], a[80:83], v[240:243], v[64:79]\n"
      "ds_read_b64_tr_b16 a[96:97], v11 offset:16384\n"
      "ds_read_b64_tr_b16 a[98:99], v11 offset:17408\n"
      "s_waitcnt lgkmcnt(4)\n"
      "s_nop 0\n"
      "v_mfma_f32_32x32x16_bf16 v[64:79], a[84:87], v[244:247], v[64:79]\n"
      "ds_read_b64_tr_b16 a[100:101], v11 offset:18432\n"
      "ds_read_b64_tr_b16 a[102:103], v11 offset:19456\n"
      "v_mfma_f32_32x32x16_bf16 v[64:79], a[88:91], v[248:251], v[64:79]\n"
      "ds_read_b64_tr_b16 a[104:105], v11 offset:20480\n"
      "ds_read_b64_tr_b16 a[106:107], v11 offset:21504\n"
      "s_waitcnt lgkmcnt(4)\n"
      "s_nop 0\n"
      "v_mfma_f32_32x32x16_bf16 v[64:79], a[92:95], v[252:255], v[64:79]\n"
      "ds_read_b64_tr_b16 a[108:109], v11 offset:22528\n"
      "ds_read_b64_tr_b16 a[110:111], v11 offset:23552\n"
      "buffer_store_dwordx4 a[44:47], v23, s[48:51], 0 offen offset:3328 sc1\n"
      "v_mfma_f32_32x32x16_bf16 v[80:95], a[96:99], v[224:227], v[80:95]\n"
      "ds_read_b64_tr_b16 a[112:113], v11 offset:24576\n"
      "ds_read_b64_tr_b16 a[114:115], v11 offset:25600\n"
      "s_waitcnt lgkmcnt(4)\n"
      "s_nop 0\n"
      "v_mfma_f32_32x32x16_bf16 v[80:95], a[100:103], v[228:231], v[80:95]\n"
      "ds_read_b64_tr_b16 a[116:117], v11 offset:26624\n"
      "ds_read_b64_tr_b16 a[118:119], v11 offset:27648\n"
      "v_mfma_f32_32x32x16_bf16 v[80:95], a[104:107], v[232:235], v[80:95]\n"
      "ds_read_b64_tr_b16 a[120:121], v11 offset:28672\n"
      "ds_read_b64_tr_b16 a[122:123], v11 offset:29696\n"
      "s_waitcnt lgkmcnt(4)\n"
      "s_nop 0\n"
      "v_mfma_f32_32x32x16_bf16 v[80:95], a[108:111], v[236:239], v[80:95]\n"
      "ds_read_b64_tr_b16 a[124:125], v11 offset:30720\n"
      "ds_read_b64_tr_b16 a[126:127], v11 offset:31744\n"
      "v_mul_f32 v64, v20, v64\n"
      "v_mul_f32 v65, v20, v65\n"
      "v_mul_f32 v66, v20, v66\n"
      "v_mul_f32 v67, v20, v67\n"
      "v_cvt_pk_bf16_f32 v64, v64, v65\n"
      "v_cvt_pk_bf16_f32 v65, v66, v67\n"
      "v_mfma_f32_32x32x16_bf16 v[80:95], a[112:115], v[240:243], v[80:95]\n"
      "ds_write_b64 v21, v[64:65] offset:0\n"
      "v_mul_f32 v68, v20, v68\n"
      "v_mul_f32 v69, v20, v69\n"
      "v_mul_f32 v70, v20, v70\n"
      "v_mul_f32 v71, v20, v71\n"
      "v_cvt_pk_bf16_f32 v66, v68, v69\n"
      "v_cvt_pk_bf16_f32 v67, v70, v71\n"
      "s_waitcnt lgkmcnt(3)\n"
      "s_nop 0\n"
      "v_mfma_f32_32x32x16_bf16 v[80:95], a[116:119], v[244:247], v[80:95]\n"
      "ds_write_b64 v21, v[66:67] offset:16\n"
      "v_mul_f32 v72, v20, v72\n"
      "v_mul_f32 v73, v20, v73\n"
      "v_mul_f32 v74, v20, v74\n"
      "v_mul_f32 v75, v20, v75\n"
      "v_mfma_f32_32x32x16_bf16 v[80:95], a[120:123], v[248:251], v[80:95]\n"
      "v_cvt_pk_bf16_f32 v68, v72, v73\n"
      "v_cvt_pk_bf16_f32 v69, v74, v75\n"
      "ds_write_b64 v21, v[68:69] offset:32\n"
      "v_mul_f32_e64 v76, v20, v76\n"
      "v_mul_f32 v77, v20, v77\n"
      "s_waitcnt lgkmcnt(3)\n"
      "v_mfma_f32_32x32x16_bf16 v[80:95], a[124:127], v[252:255], v[80:95]\n"
      "v_mul_f32 v78, v20, v78\n"
      "v_mul_f32 v79, v20, v79\n"
      "v_cvt_pk_bf16_f32 v70, v76, v77\n"
      "v_cvt_pk_bf16_f32 v71, v78, v79\n"
      "ds_write_b64 v21, v[70:71] offset:48\n"
      "s_nop 6\n"
      "v_mul_f32 v80, v20, v80\n"
      "v_mul_f32 v81, v20, v81\n"
      "v_mul_f32 v82, v20, v82\n"
      "v_mul_f32_e64 v83, v20, v83\n"
      "v_cvt_pk_bf16_f32 v72, v80, v81\n"
      "v_cvt_pk_bf16_f32 v73, v82, v83\n"
      "ds_write_b64 v21, v[72:73] offset:64\n"
      "v_mul_f32 v84, v20, v84\n"
      "v_mul_f32 v85, v20, v85\n"
      "v_mul_f32 v86, v20, v86\n"
      "v_mul_f32 v87, v20, v87\n"
      "v_cvt_pk_bf16_f32 v74, v84, v85\n"
      "v_cvt_pk_bf16_f32 v75, v86, v87\n"
      "ds_write_b64 v21, v[74:75] offset:80\n"
      "v_mul_f32 v88, v20, v88\n"
      "v_mul_f32 v89, v20, v89\n"
      "v_mul_f32 v90, v20, v90\n"
      "v_mul_f32 v91, v20, v91\n"
      "v_cvt_pk_bf16_f32 v76, v88, v89\n"
      "v_cvt_pk_bf16_f32 v77, v90, v91\n"
      "ds_write_b64 v21, v[76:77] offset:96\n"
      "v_mul_f32 v92, v20, v92\n"
      "v_mul_f32 v93, v20, v93\n"
      "v_mul_f32 v94, v20, v94\n"
      "v_mul_f32 v95, v20, v95\n"
      "v_cvt_pk_bf16_f32 v78, v92, v93\n"
      "v_cvt_pk_bf16_f32 v79, v94, v95\n"
      ".Lpad5_%=:\n"
      "s_cbranch_execz .LpadretB_%=\n"
      "s_nop 0\n"
      "ds_write_b64 v21, v[78:79] offset:112\n"
      "ds_read_b64 a[48:49], v22 offset:0\n"
      "ds_read_b64 a[50:51], v24 offset:0\n"
      "ds_read_b64 a[52:53], v22 offset:288\n"
      "ds_read_b64 a[54:55], v24 offset:288\n"
      "ds_read_b64 a[56:57], v22 offset:576\n"
      "ds_read_b64 a[58:59], v24 offset:576\n"
      "ds_read_b64 a[60:61], v22 offset:864\n"
      "ds_read_b64 a[62:63], v24 offset:864\n"
      "s_waitcnt lgkmcnt(6)\n"
      "s_nop 0\n"
      "buffer_store_dwordx4 a[48:51], v23, s[48:51], 0 offen offset:384 sc1\n"
      "s_waitcnt lgkmcnt(4)\n"
      "s_nop 0\n"
      "buffer_store_dwordx4 a[52:55], v23, s[48:51], 0 offen offset:1408 sc1\n"
      "s_waitcnt lgkmcnt(2)\n"
      "s_nop 0\n"
      "buffer_store_dwordx4 a[56:59], v23, s[48:51], 0 offen offset:2432 sc1\n"
      "s_waitcnt lgkmcnt(0)\n"
      "s_nop 0\n"
      "buffer_store_dwordx4 a[60:63], v23, s[48:51], 0 offen offset:3456 sc1\n"
      "s_endpgm\n"
      ".p2align 3\n"
      ".Lslow_%=:\n"
      "s_waitcnt vmcnt(8)\n"
      "s_barrier\n"
      "s_nop 4\n"
      "s_nop 0\n"
      "v_mfma_f32_32x32x16_bf16 v[112:127], a[96:99], a[32:35], v[112:127]\n"
      "ds_read_b128 a[64:67], v72 offset:0\n"
      "v_add_f32_e64 v16, v16, v56\n"
      "v_cvt_pk_bf16_f32 v139, v54, v55\n"
      "v_fma_f32 v59, v59, s54, -v14\n"
      "v_exp_f32 v58, v58\n"
      "v_add_f32 v17, v17, v57\n"
      "v_fma_f32 v60, v60, s54, -v14\n"
      "v_exp_f32 v59, v59\n"
      "s_nop 4\n"
      "v_mfma_f32_32x32x16_bf16 v[112:127], a[100:103], a[36:39], v[112:127]\n"
      "ds_read_b128 a[68:71], v73 offset:0\n"
      "s_add_u32 m0, s58, 131072\n"
      "s_mov_b32 s61, 131072\n"
      "v_add_f32_e64 v16, v16, v58\n"
      "v_cvt_pk_bf16_f32 v140, v56, v57\n"
      "v_fma_f32 v61, v61, s54, -v14\n"
      "v_exp_f32_e64 v60, v60\n"
      "v_add_f32 v17, v17, v59\n"
      "s_nop 4\n"
      "v_mfma_f32_32x32x16_bf16 v[112:127], a[104:107], a[40:43], v[112:127]\n"
      "ds_read_b128 a[72:75], v74 offset:0\n"
      "buffer_load_dwordx4 v2, s[40:43], s61 offen offset:0 nt lds\n"
      "v_fma_f32 v62, v62, s54, -v14\n"
      "v_exp_f32 v61, v61\n"
      "v_add_f32 v16, v16, v60\n"
      "v_cvt_pk_bf16_f32 v141, v58, v59\n"
      "v_fma_f32 v63, v63, s54, -v14\n"
      "v_exp_f32 v62, v62\n"
      "s_nop 4\n"
      "v_mfma_f32_32x32x16_bf16 v[112:127], a[108:111], a[44:47], v[112:127]\n"
      "ds_read_b128 a[76:79], v75 offset:0\n"
      "v_add_f32 v17, v17, v61\n"
      "v_exp_f32 v63, v63\n"
      "v_add_f32_e64 v16, v16, v62\n"
      "v_cvt_pk_bf16_f32 v142, v60, v61\n"
      "v_add_f32_e64 v17, v17, v63\n"
      "v_cvt_pk_bf16_f32 v143, v62, v63\n"
      "s_nop 4\n"
      "s_nop 0\n"
      "v_mfma_f32_32x32x16_bf16 v[112:127], a[112:115], a[48:51], v[112:127]\n"
      "ds_read_b128 a[80:83], v76 offset:0\n"
      "v_max3_f32 v18, v96, v97, v98\n"
      "v_max3_f32 v19, v103, v104, v105\n"
      "v_max3_f32 v18, v18, v99, v100\n"
      "v_max3_f32 v19, v19, v106, v107\n"
      "v_max3_f32 v18, v18, v101, v102\n"
      "v_max3_f32 v19, v19, v108, v109\n"
      "v_max3_f32 v19, v19, v110, v111\n"
      "v_max_f32 v18, v18, v19\n"
      "s_nop 4\n"
      "v_mfma_f32_32x32x16_bf16 v[112:127], a[116:119], a[52:55], v[112:127]\n"
      "ds_read_b128 a[84:87], v77 offset:0\n"
      "buffer_load_dwordx4 v3, s[40:43], s61 offen offset:1024 nt lds\n"
      "v_cmp_gt_f32 vcc, v18, v15\n"
      "s_cbranch_vccnz .Lrares1_%=\n"
      ".Lrets1_%=:\n"
      "v_fma_f32 v96, v96, s54, -v14\n"
      "v_fma_f32 v97, v97, s54, -v14\n"
      "v_exp_f32 v96, v96\n"
      "s_nop 4\n"
      "v_mfma_f32_32x32x16_bf16 v[112:127], a[120:123], a[56:59], v[112:127]\n"
      "ds_read_b128 a[88:91], v78 offset:0\n"
      "v_fma_f32 v98, v98, s54, -v14\n"
      "v_exp_f32 v97, v97\n"
      "v_add_f32 v16, v16, v96\n"
      "v_fma_f32 v99, v99, s54, -v14\n"
      "v_exp_f32_e64 v98, v98\n"
      "v_add_f32 v17, v17, v97\n"
      "s_nop 4\n"
      "v_mfma_f32_32x32x16_bf16 v[112:127], a[124:127], a[60:63], v[112:127]\n"
      "ds_read_b128 a[92:95], v79 offset:0\n"
      "v_fma_f32 v100, v100, s54, -v14\n"
      "v_exp_f32 v99, v99\n"
      "v_add_f32 v16, v16, v98\n"
      "v_cvt_pk_bf16_f32 v144, v96, v97\n"
      "v_fma_f32 v101, v101, s54, -v14\n"
      "s_nop 4\n"
      "s_waitcnt lgkmcnt(4)\n"
      "v_mfma_f32_32x32x16_bf16 v[32:47], a[64:67], a[0:3], 0\n"
      "ds_read_b128 a[96:99], v72 offset:256\n"
      "buffer_load_dwordx4 v4, s[40:43], s61 offen offset:2048 nt lds\n"
      "v_exp_f32 v100, v100\n"
      "v_add_f32 v17, v17, v99\n"
      "v_fma_f32 v102, v102, s54, -v14\n"
      "v_exp_f32_e64 v101, v101\n"
      "v_add_f32 v16, v16, v100\n"
      "s_nop 3\n"
      "v_mfma_f32_32x32x16_bf16 v[32:47], a[68:71], a[4:7], v[32:47]\n"
      "ds_read_b128 a[100:103], v73 offset:256\n"
      "v_cvt_pk_bf16_f32 v145, v98, v99\n"
      "v_fma_f32 v103, v103, s54, -v14\n"
      "v_exp_f32 v102, v102\n"
      "v_add_f32 v17, v17, v101\n"
      "v_fma_f32 v104, v104, s54, -v14\n"
      "v_exp_f32 v103, v103\n"
      "s_nop 3\n"
      "v_mfma_f32_32x32x16_bf16 v[32:47], a[72:75], a[8:11], v[32:47]\n"
      "ds_read_b128 a[104:107], v74 offset:256\n"
      "v_add_f32_e64 v16, v16, v102\n"
      "v_cvt_pk_bf16_f32 v146, v100, v101\n"
      "v_fma_f32 v105, v105, s54, -v14\n"
      "v_exp_f32 v104, v104\n"
      "v_add_f32 v17, v17, v103\n"
      "v_fma_f32 v106, v106, s54, -v14\n"
      "s_nop 3\n"
      "s_nop 0\n"
      "v_mfma_f32_32x32x16_bf16 v[32:47], a[76:79], a[12:15], v[32:47]\n"
      "ds_read_b128 a[108:111], v75 offset:256\n"
      "buffer_load_dwordx4 v5, s[40:43], s61 offen offset:3072 nt lds\n"
      "v_exp_f32 v105, v105\n"
      "v_add_f32 v16, v16, v104\n"
      "v_cvt_pk_bf16_f32 v147, v102, v103\n"
      "v_fma_f32 v107, v107, s54, -v14\n"
      "s_nop 3\n"
      "s_waitcnt lgkmcnt(4)\n"
      "v_mfma_f32_32x32x16_bf16 v[32:47], a[80:83], a[16:19], v[32:47]\n"
      "ds_read_b128 a[112:115], v76 offset:256\n"
      "v_exp_f32 v106, v106\n"
      "v_add_f32 v17, v17, v105\n"
      "v_fma_f32 v108, v108, s54, -v14\n"
      "v_exp_f32 v107, v107\n"
      "v_add_f32 v16, v16, v106\n"
      "v_cvt_pk_bf16_f32 v148, v104, v105\n"
      "s_nop 3\n"
      "s_nop 0\n"
      "v_mfma_f32_32x32x16_bf16 v[32:47], a[84:87], a[20:23], v[32:47]\n"
      "ds_read_b128 a[116:119], v77 offset:256\n"
      "v_fma_f32 v109, v109, s54, -v14\n"
      "v_exp_f32 v108, v108\n"
      "v_add_f32 v17, v17, v107\n"
      "v_fma_f32 v110, v110, s54, -v14\n"
      "v_exp_f32_e64 v109, v109\n"
      "v_add_f32 v16, v16, v108\n"
      "s_nop 3\n"
      "v_mfma_f32_32x32x16_bf16 v[32:47], a[88:91], a[24:27], v[32:47]\n"
      "ds_read_b128 a[120:123], v78 offset:256\n"
      "s_add_u32 m0, s58, 147456\n"
      "s_mov_b32 s61, 147456\n"
      "v_cvt_pk_bf16_f32 v149, v106, v107\n"
      "v_fma_f32 v111, v111, s54, -v14\n"
      "v_exp_f32 v110, v110\n"
      "v_add_f32 v17, v17, v109\n"
      "v_exp_f32 v111, v111\n"
      "s_nop 3\n"
      "v_mfma_f32_32x32x16_bf16 v[32:47], a[92:95], a[28:31], v[32:47]\n"
      "ds_read_b128 a[124:127], v79 offset:256\n"
      "buffer_load_dwordx4 v2, s[40:43], s61 offen offset:0 nt lds\n"
      "v_add_f32_e64 v16, v16, v110\n"
      "v_cvt_pk_bf16_f32 v150, v108, v109\n"
      "v_add_f32_e64 v17, v17, v111\n"
      "v_cvt_pk_bf16_f32 v151, v110, v111\n"
      "s_nop 3\n"
      "s_waitcnt lgkmcnt(4)\n"
      "v_mfma_f32_32x32x16_bf16 v[32:47], a[96:99], a[32:35], v[32:47]\n"
      "ds_read_b128 a[64:67], v72 offset:16384\n"
      "v_max3_f32 v18, v112, v113, v114\n"
      "v_max3_f32 v19, v119, v120, v121\n"
      "v_max3_f32 v18, v18, v115, v116\n"
      "v_max3_f32 v19, v19, v122, v123\n"
      "v_max3_f32 v18, v18, v117, v118\n"
      "v_max3_f32 v19, v19, v124, v125\n"
      "v_max3_f32 v19, v19, v126, v127\n"
      "v_max_f32_e64 v18, v18, v19\n"
      "v_cmp_gt_f32 vcc, v18, v15\n"
      "s_nop 3\n"
      "v_mfma_f32_32x32x16_bf16 v[32:47], a[100:103], a[36:39], v[32:47]\n"
      "ds_read_b128 a[68:71], v73 offset:16384\n"
      "s_cbranch_vccnz .Lrares2_%=\n"
      ".Lrets2_%=:\n"
      "s_nop 0\n"
      "v_fma_f32 v112, v112, s54, -v14\n"
      "v_fma_f32 v113, v113, s54, -v14\n"
      "v_exp_f32_e64 v112, v112\n"
      "v_fma_f32 v114, v114, s54, -v14\n"
      "v_exp_f32_e64 v113, v113\n"
      "v_add_f32 v16, v16, v112\n"
      "s_nop 3\n"
      "v_mfma_f32_32x32x16_bf16 v[32:47], a[104:107], a[40:43], v[32:47]\n"
      "ds_read_b128 a[72:75], v74 offset:16384\n"
      "buffer_load_dwordx4 v3, s[40:43], s61 offen offset:1024 nt lds\n"
      "v_fma_f32 v115, v115, s54, -v14\n"
      "v_exp_f32 v114, v114\n"
      "v_add_f32 v17, v17, v113\n"
      "v_fma_f32 v116, v116, s54, -v14\n"
      "v_exp_f32 v115, v115\n"
      "s_nop 3\n"
      "v_mfma_f32_32x32x16_bf16 v[32:47], a[108:111], a[44:47], v[32:47]\n"
      "ds_read_b128 a[76:79], v75 offset:16384\n"
      "v_add_f32_e64 v16, v16, v114\n"
      "v_cvt_pk_bf16_f32 v152, v112, v113\n"
      "v_fma_f32 v117, v117, s54, -v14\n"
      "v_exp_f32 v116, v116\n"
      "v_add_f32 v17, v17, v115\n"
      "v_fma_f32 v118, v118, s54, -v14\n"
      "s_nop 3\n"
      "s_waitcnt lgkmcnt(4)\n"
      "v_mfma_f32_32x32x16_bf16 v[32:47], a[112:115], a[48:51], v[32:47]\n"
      "ds_read_b128 a[80:83], v76 offset:16384\n"
      "v_exp_f32 v117, v117\n"
      "v_add_f32 v16, v16, v116\n"
      "v_cvt_pk_bf16_f32 v153, v114, v115\n"
      "v_fma_f32 v119, v119, s54, -v14\n"
      "v_exp_f32 v118, v118\n"
      "v_add_f32 v17, v17, v117\n"
      "v_fma_f32 v120, v120, s54, -v14\n"
      "s_nop 3\n"
      "s_nop 0\n"
      "v_mfma_f32_32x32x16_bf16 v[32:47], a[116:119], a[52:55], v[32:47]\n"
      "ds_read_b128 a[84:87], v77 offset:16384\n"
      "v_exp_f32 v119, v119\n"
      "v_add_f32 v16, v16, v118\n"
      "v_cvt_pk_bf16_f32 v154, v116, v117\n"
      "v_fma_f32 v121, v121, s54, -v14\n"
      "v_exp_f32 v120, v120\n"
      "v_add_f32 v17, v17, v119\n"
      "v_fma_f32 v122, v122, s54, -v14\n"
      "s_nop 3\n"
      "s_nop 0\n"
      "v_mfma_f32_32x32x16_bf16 v[32:47], a[120:123], a[56:59], v[32:47]\n"
      "ds_read_b128 a[88:91], v78 offset:16384\n"
      "buffer_load_dwordx4 v4, s[40:43], s61 offen offset:2048 nt lds\n"
      "v_exp_f32 v121, v121\n"
      "v_add_f32 v16, v16, v120\n"
      "v_cvt_pk_bf16_f32 v155, v118, v119\n"
      "v_fma_f32 v123, v123, s54, -v14\n"
      "v_exp_f32 v122, v122\n"
      "s_nop 3\n"
      "v_mfma_f32_32x32x16_bf16 v[32:47], a[124:127], a[60:63], v[32:47]\n"
      "ds_read_b128 a[92:95], v79 offset:16384\n"
      "v_add_f32_e64 v17, v17, v121\n"
      "v_fma_f32 v124, v124, s54, -v14\n"
      "v_exp_f32 v123, v123\n"
      "v_add_f32 v16, v16, v122\n"
      "v_cvt_pk_bf16_f32 v156, v120, v121\n"
      "v_fma_f32 v125, v125, s54, -v14\n"
      "s_nop 3\n"
      "s_waitcnt lgkmcnt(4)\n"
      "v_mfma_f32_32x32x16_bf16 v[48:63], a[64:67], a[0:3], 0\n"
      "ds_read_b128 a[96:99], v72 offset:16640\n"
      "v_exp_f32 v124, v124\n"
      "v_add_f32 v17, v17, v123\n"
      "v_fma_f32 v126, v126, s54, -v14\n"
      "v_exp_f32 v125, v125\n"
      "v_add_f32 v16, v16, v124\n"
      "v_cvt_pk_bf16_f32 v157, v122, v123\n"
      "v_fma_f32 v127, v127, s54, -v14\n"
      "s_nop 2\n"
      "s_nop 0\n"
      "v_mfma_f32_32x32x16_bf16 v[48:63], a[68:71], a[4:7], v[48:63]\n"
      "ds_read_b128 a[100:103], v73 offset:16640\n"
      "v_exp_f32 v126, v126\n"
      "v_add_f32 v17, v17, v125\n"
      "v_exp_f32 v127, v127\n"
      "v_add_f32 v16, v16, v126\n"
      "v_cvt_pk_bf16_f32 v158, v124, v125\n"
      "v_add_f32_e64 v17, v17, v127\n"
      "v_cvt_pk_bf16_f32 v159, v126, v127\n"
      "s_nop 3\n"
      "s_nop 0\n"
      "v_mfma_f32_32x32x16_bf16 v[48:63], a[72:75], a[8:11], v[48:63]\n"
      "ds_read_b128 a[104:107], v74 offset:16640\n"
      "buffer_load_dwordx4 v5, s[40:43], s61 offen offset:3072 nt lds\n"
      "v_max3_f32 v18, v32, v33, v34\n"
      "v_max3_f32 v19, v39, v40, v41\n"
      "v_max3_f32 v18, v18, v35, v36\n"
      "v_max3_f32 v19, v19, v42, v43\n"
      "s_nop 2\n"
      "s_nop 0\n"
      "v_mfma_f32_32x32x16_bf16 v[48:63], a[76:79], a[12:15], v[48:63]\n"
      "ds_read_b128 a[108:111], v75 offset:16640\n"
      "v_max3_f32 v18, v18, v37, v38\n"
      "v_max3_f32 v19, v19, v44, v45\n"
      "v_max3_f32 v19, v19, v46, v47\n"
      "v_max_f32_e64 v18, v18, v19\n"
      "v_cmp_gt_f32 vcc, v18, v15\n"
      "s_cbranch_vccnz .Lrares3_%=\n"
      ".Lrets3_%=:\n"
      "s_nop 3\n"
      "s_waitcnt lgkmcnt(4)\n"
      "v_mfma_f32_32x32x16_bf16 v[48:63], a[80:83], a[16:19], v[48:63]\n"
      "ds_read_b128 a[112:115], v76 offset:16640\n"
      "v_fma_f32 v32, v32, s54, -v14\n"
      "v_fma_f32 v33, v33, s54, -v14\n"
      "v_exp_f32_e64 v32, v32\n"
      "v_fma_f32 v34, v34, s54, -v14\n"
      "v_exp_f32 v33, v33\n"
      "s_nop 2\n"
      "v_mfma_f32_32x32x16_bf16 v[48:63], a[84:87], a[20:23], v[48:63]\n"
      "ds_read_b128 a[116:119], v77 offset:16640\n"
      "v_add_f32_e64 v16, v16, v32\n"
      "v_fma_f32 v35, v35, s54, -v14\n"
      "v_exp_f32 v34, v34\n"
      "v_add_f32 v17, v17, v33\n"
      "v_fma_f32 v36, v36, s54, -v14\n"
      "s_nop 3\n"
      "s_nop 0\n"
      "v_mfma_f32_32x32x16_bf16 v[48:63], a[88:91], a[24:27], v[48:63]\n"
      "ds_read_b128 a[120:123], v78 offset:16640\n"
      "v_exp_f32 v35, v35\n"
      "v_add_f32 v16, v16, v34\n"
      "v_cvt_pk_bf16_f32 v160, v32, v33\n"
      "v_fma_f32 v37, v37, s54, -v14\n"
      "v_exp_f32 v36, v36\n"
      "s_nop 2\n"
      "v_mfma_f32_32x32x16_bf16 v[48:63], a[92:95], a[28:31], v[48:63]\n"
      "ds_read_b128 a[124:127], v79 offset:16640\n"
      "s_waitcnt vmcnt(8)\n"
      "s_barrier\n"
      "s_nop 3\n"
      "s_waitcnt lgkmcnt(4)\n"
      "v_mfma_f32_32x32x16_bf16 v[48:63], a[96:99], a[32:35], v[48:63]\n"
      "ds_read_b128 a[64:67], v72 offset:32768\n"
      "s_add_u32 m0, s58, 0\n"
      "s_nop 0\n"
      "s_mov_b32 s61, 163840\n"
      "buffer_load_dwordx4 v2, s[40:43], s61 offen offset:0 nt lds\n"
      "v_add_f32_e64 v17, v17, v35\n"
      "v_fma_f32 v38, v38, s54, -v14\n"
      "s_nop 2\n"
      "s_nop 0\n"
      "v_mfma_f32_32x32x16_bf16 v[48:63], a[100:103], a[36:39], v[48:63]\n"
      "ds_read_b128 a[68:71], v73 offset:32768\n"
      "v_exp_f32 v37, v37\n"
      "v_add_f32 v16, v16, v36\n"
      "v_cvt_pk_bf16_f32 v161, v34, v35\n"
      "v_fma_f32 v39, v39, s54, -v14\n"
      "v_exp_f32 v38, v38\n"
      "s_nop 3\n"
      "v_mfma_f32_32x32x16_bf16 v[48:63], a[104:107], a[40:43], v[48:63]\n"
      "ds_read_b128 a[72:75], v74 offset:32768\n"
      "buffer_load_dwordx4 v3, s[40:43], s61 offen offset:1024 nt lds\n"
      "v_add_f32_e64 v17, v17, v37\n"
      "v_fma_f32 v40, v40, s54, -v14\n"
      "v_exp_f32 v39, v39\n"
      "s_nop 2\n"
      "v_mfma_f32_32x32x16_bf16 v[48:63], a[108:111], a[44:47], v[48:63]\n"
      "ds_read_b128 a[76:79], v75 offset:32768\n"
      "v_add_f32_e64 v16, v16, v38\n"
      "v_cvt_pk_bf16_f32 v162, v36, v37\n"
      "v_fma_f32 v41, v41, s54, -v14\n"
      "v_exp_f32_e64 v40, v40\n"
      "s_nop 3\n"
      "s_waitcnt lgkmcnt(4)\n"
      "v_mfma_f32_32x32x16_bf16 v[48:63], a[112:115], a[48:51], v[48:63]\n"
      "ds_read_b128 a[80:83], v76 offset:32768\n"
      "v_add_f32_e64 v17, v17, v39\n"
      "v_fma_f32 v42, v42, s54, -v14\n"
      "v_exp_f32 v41, v41\n"
      "v_add_f32 v16, v16, v40\n"
      "v_cvt_pk_bf16_f32 v163, v38, v39\n"
      "v_fma_f32 v43, v43, s54, -v14\n"
      "s_nop 2\n"
      "s_nop 0\n"
      "v_mfma_f32_32x32x16_bf16 v[48:63], a[116:119], a[52:55], v[48:63]\n"
      "ds_read_b128 a[84:87], v77 offset:32768\n"
      "buffer_load_dwordx4 v4, s[40:43], s61 offen offset:2048 nt lds\n"
      "v_exp_f32 v42, v42\n"
      "v_add_f32 v17, v17, v41\n"
      "v_fma_f32 v44, v44, s54, -v14\n"
      "s_nop 3\n"
      "s_nop 0\n"
      "v_mfma_f32_32x32x16_bf16 v[48:63], a[120:123], a[56:59], v[48:63]\n"
      "ds_read_b128 a[88:91], v78 offset:32768\n"
      "v_exp_f32 v43, v43\n"
      "v_add_f32 v16, v16, v42\n"
      "v_cvt_pk_bf16_f32 v164, v40, v41\n"
      "v_fma_f32 v45, v45, s54, -v14\n"
      "v_exp_f32 v44, v44\n"
      "s_nop 2\n"
      "v_mfma_f32_32x32x16_bf16 v[48:63], a[124:127], a[60:63], v[48:63]\n"
      "ds_read_b128 a[92:95], v79 offset:32768\n"
      "v_add_f32_e64 v17, v17, v43\n"
      "v_fma_f32 v46, v46, s54, -v14\n"
      "v_exp_f32 v45, v45\n"
      "v_add_f32 v16, v16, v44\n"
      "s_nop 3\n"
      "s_waitcnt lgkmcnt(4)\n"
      "v_mfma_f32_32x32x16_bf16 v[96:111], a[64:67], a[0:3], 0\n"
      "ds_read_b128 a[96:99], v72 offset:33024\n"
      "buffer_load_dwordx4 v5, s[40:43], s61 offen offset:3072 nt lds\n"
      "v_cvt_pk_bf16_f32 v165, v42, v43\n"
      "v_fma_f32 v47, v47, s54, -v14\n"
      "v_exp_f32_e64 v46, v46\n"
      "v_add_f32 v17, v17, v45\n"
      "s_nop 1\n"
      "v_mfma_f32_32x32x16_bf16 v[96:111], a[68:71], a[4:7], v[96:111]\n"
      "ds_read_b128 a[100:103], v73 offset:33024\n"
      "v_exp_f32 v47, v47\n"
      "v_add_f32 v16, v16, v46\n"
      "v_cvt_pk_bf16_f32 v166, v44, v45\n"
      "v_add_f32_e64 v17, v17, v47\n"
      "v_cvt_pk_bf16_f32 v167, v46, v47\n"
      "s_nop 2\n"
      "s_nop 0\n"
      "v_mfma_f32_32x32x16_bf16 v[96:111], a[72:75], a[8:11], v[96:111]\n"
      "ds_read_b128 a[104:107], v74 offset:33024\n"
      "v_max3_f32 v18, v48, v49, v50\n"
      "v_max3_f32 v19, v55, v56, v57\n"
      "v_max3_f32 v18, v18, v51, v52\n"
      "v_max3_f32 v19, v19, v58, v59\n"
      "v_max3_f32 v18, v18, v53, v54\n"
      "v_max3_f32 v19, v19, v60, v61\n"
      "s_nop 1\n"
      "s_nop 0\n"
      "v_mfma_f32_32x32x16_bf16 v[96:111], a[76:79], a[12:15], v[96:111]\n"
      "ds_read_b128 a[108:111], v75 offset:33024\n"
      "v_max3_f32 v19, v19, v62, v63\n"
      "v_max_f32_e64 v18, v18, v19\n"
      "v_cmp_gt_f32 vcc, v18, v15\n"
      "s_cbranch_vccnz .Lrares4_%=\n"
      ".Lrets4_%=:\n"
      "v_fma_f32 v48, v48, s54, -v14\n"
      "s_nop 2\n"
      "s_waitcnt lgkmcnt(4)\n"
      "v_mfma_f32_32x32x16_bf16 v[96:111], a[80:83], a[16:19], v[96:111]\n"
      "ds_read_b128 a[112:115], v76 offset:33024\n"
      "s_add_u32 m0, s58, 16384\n"
      "s_mov_b32 s61, 180224\n"
      "v_fma_f32 v49, v49, s54, -v14\n"
      "v_exp_f32_e64 v48, v48\n"
      "v_fma_f32 v50, v50, s54, -v14\n"
      "s_nop 1\n"
      "s_nop 0\n"
      "v_mfma_f32_32x32x16_bf16 v[96:111], a[84:87], a[20:23], v[96:111]\n"
      "ds_read_b128 a[116:119], v77 offset:33024\n"
      "buffer_load_dwordx4 v2, s[40:43], s61 offen offset:0 nt lds\n"
      "v_exp_f32 v49, v49\n"
      "v_add_f32 v16, v16, v48\n"
      "v_fma_f32 v51, v51, s54, -v14\n"
      "s_nop 2\n"
      "s_nop 0\n"
      "v_mfma_f32_32x32x16_bf16 v[96:111], a[88:91], a[24:27], v[96:111]\n"
      "ds_read_b128 a[120:123], v78 offset:33024\n"
      "v_exp_f32 v50, v50\n"
      "v_add_f32 v17, v17, v49\n"
      "v_fma_f32 v52, v52, s54, -v14\n"
      "v_exp_f32_e64 v51, v51\n"
      "v_add_f32 v16, v16, v50\n"
      "s_nop 1\n"
      "v_mfma_f32_32x32x16_bf16 v[96:111], a[92:95], a[28:31], v[96:111]\n"
      "ds_read_b128 a[124:127], v79 offset:33024\n"
      "v_cvt_pk_bf16_f32 v168, v48, v49\n"
      "v_fma_f32 v53, v53, s54, -v14\n"
      "v_exp_f32 v52, v52\n"
      "v_add_f32 v17, v17, v51\n"
      "s_nop 2\n"
      "s_waitcnt lgkmcnt(4)\n"
      "v_mfma_f32_32x32x16_bf16 v[96:111], a[96:99], a[32:35], v[96:111]\n"
      "ds_read_b128 a[64:67], v72 offset:49152\n"
      "buffer_load_dwordx4 v3, s[40:43], s61 offen offset:1024 nt lds\n"
      "v_fma_f32 v54, v54, s54, -v14\n"
      "v_exp_f32_e64 v53, v53\n"
      "v_add_f32 v16, v16, v52\n"
      "s_nop 1\n"
      "v_mfma_f32_32x32x16_bf16 v[96:111], a[100:103], a[36:39], v[96:111]\n"
      "ds_read_b128 a[68:71], v73 offset:49152\n"
      "v_cvt_pk_bf16_f32 v169, v50, v51\n"
      "v_fma_f32 v55, v55, s54, -v14\n"
      "v_exp_f32 v54, v54\n"
      "v_add_f32 v17, v17, v53\n"
      "v_fma_f32 v56, v56, s54, -v14\n"
      "s_nop 2\n"
      "s_nop 0\n"
      "v_mfma_f32_32x32x16_bf16 v[96:111], a[104:107], a[40:43], v[96:111]\n"
      "ds_read_b128 a[72:75], v74 offset:49152\n"
      "v_exp_f32 v55, v55\n"
      "v_add_f32 v16, v16, v54\n"
      "v_cvt_pk_bf16_f32 v170, v52, v53\n"
      "v_fma_f32 v57, v57, s54, -v14\n"
      "v_exp_f32 v56, v56\n"
      "s_nop 1\n"
      "v_mfma_f32_32x32x16_bf16 v[96:111], a[108:111], a[44:47], v[96:111]\n"
      "ds_read_b128 a[76:79], v75 offset:49152\n"
      "buffer_load_dwordx4 v4, s[40:43], s61 offen offset:2048 nt lds\n"
      "v_add_f32_e64 v17, v17, v55\n"
      "v_fma_f32 v58, v58, s54, -v14\n"
      "s_nop 2\n"
      "s_waitcnt lgkmcnt(4)\n"
      "v_mfma_f32_32x32x16_bf16 v[96:111], a[112:115], a[48:51], v[96:111]\n"
      "ds_read_b128 a[80:83], v76 offset:49152\n"
      "v_exp_f32 v57, v57\n"
      "v_add_f32 v16, v16, v56\n"
      "v_cvt_pk_bf16_f32 v171, v54, v55\n"
      "v_fma_f32 v59, v59, s54, -v14\n"
      "v_exp_f32 v58, v58\n"
      "s_nop 1\n"
      "v_mfma_f32_32x32x16_bf16 v[96:111], a[116:119], a[52:55], v[96:111]\n"
      "ds_read_b128 a[84:87], v77 offset:49152\n"
      "v_add_f32_e64 v17, v17, v57\n"
      "v_fma_f32 v60, v60, s54, -v14\n"
      "v_exp_f32 v59, v59\n"
      "v_add_f32 v16, v16, v58\n"
      "v_cvt_pk_bf16_f32 v172, v56, v57\n"
      "s_nop 2\n"
      "s_nop 0\n"
      "v_mfma_f32_32x32x16_bf16 v[96:111], a[120:123], a[56:59], v[96:111]\n"
      "ds_read_b128 a[88:91], v78 offset:49152\n"
      "buffer_load_dwordx4 v5, s[40:43], s61 offen offset:3072 nt lds\n"
      "v_fma_f32 v61, v61, s54, -v14\n"
      "v_exp_f32_e64 v60, v60\n"
      "v_add_f32 v17, v17, v59\n"
      "s_nop 1\n"
      "v_mfma_f32_32x32x16_bf16 v[96:111], a[124:127], a[60:63], v[96:111]\n"
      "ds_read_b128 a[92:95], v79 offset:49152\n"
      "v_fma_f32 v62, v62, s54, -v14\n"
      "v_exp_f32 v61, v61\n"
      "v_add_f32 v16, v16, v60\n"
      "v_cvt_pk_bf16_f32 v173, v58, v59\n"
      "s_nop 2\n"
      "s_waitcnt lgkmcnt(4)\n"
      "v_mfma_f32_32x32x16_bf16 v[112:127], a[64:67], a[0:3], 0\n"
      "ds_read_b128 a[96:99], v72 offset:49408\n"
      "v_fma_f32 v63, v63, s54, -v14\n"
      "v_exp_f32 v62, v62\n"
      "v_add_f32 v17, v17, v61\n"
      "v_exp_f32 v63, v63\n"
      "s_nop 1\n"
      "v_mfma_f32_32x32x16_bf16 v[112:127], a[68:71], a[4:7], v[112:127]\n"
      "ds_read_b128 a[100:103], v73 offset:49408\n"
      "s_add_u32 m0, s58, 32768\n"
      "s_mov_b32 s61, 0\n"
      "v_add_f32 v16, v16, v62\n"
      "v_cvt_pk_bf16_f32 v174, v60, v61\n"
      "v_add_f32_e64 v17, v17, v63\n"
      "v_cvt_pk_bf16_f32 v175, v62, v63\n"
      "s_nop 1\n"
      "s_nop 0\n"
      "v_mfma_f32_32x32x16_bf16 v[112:127], a[72:75], a[8:11], v[112:127]\n"
      "ds_read_b128 a[104:107], v74 offset:49408\n"
      "buffer_load_dwordx4 v6, s[44:47], s61 offen offset:0 nt lds\n"
      "v_max3_f32 v18, v96, v97, v98\n"
      "v_max3_f32 v19, v103, v104, v105\n"
      "v_max3_f32 v18, v18, v99, v100\n"
      "v_max3_f32 v19, v19, v106, v107\n"
      "v_max3_f32 v18, v18, v101, v102\n"
      "s_nop 1\n"
      "s_nop 0\n"
      "v_mfma_f32_32x32x16_bf16 v[112:127], a[76:79], a[12:15], v[112:127]\n"
      "ds_read_b128 a[108:111], v75 offset:49408\n"
      "v_max3_f32 v19, v19, v108, v109\n"
      "v_max3_f32 v19, v19, v110, v111\n"
      "v_max_f32_e64 v18, v18, v19\n"
      "v_cmp_gt_f32 vcc, v18, v15\n"
      "s_cbranch_vccnz .Lrares5_%=\n"
      ".Lrets5_%=:\n"
      "s_nop 1\n"
      "s_waitcnt lgkmcnt(4)\n"
      "v_mfma_f32_32x32x16_bf16 v[112:127], a[80:83], a[16:19], v[112:127]\n"
      "ds_read_b128 a[112:115], v76 offset:49408\n"
      "buffer_load_dwordx4 v7, s[44:47], s61 offen offset:1024 nt lds\n"
      "v_fma_f32 v96, v96, s54, -v14\n"
      "v_fma_f32 v97, v97, s54, -v14\n"
      "v_exp_f32_e64 v96, v96\n"
      "v_fma_f32 v98, v98, s54, -v14\n"
      "s_nop 1\n"
      "s_nop 0\n"
      "v_mfma_f32_32x32x16_bf16 v[112:127], a[84:87], a[20:23], v[112:127]\n"
      "ds_read_b128 a[116:119], v77 offset:49408\n"
      "v_exp_f32 v97, v97\n"
      "v_add_f32 v16, v16, v96\n"
      "v_fma_f32 v99, v99, s54, -v14\n"
      "v_exp_f32 v98, v98\n"
      "s_nop 1\n"
      "v_mfma_f32_32x32x16_bf16 v[112:127], a[88:91], a[24:27], v[112:127]\n"
      "ds_read_b128 a[120:123], v78 offset:49408\n"
      "v_add_f32_e64 v17, v17, v97\n"
      "v_fma_f32 v100, v100, s54, -v14\n"
      "v_exp_f32 v99, v99\n"
      "v_add_f32 v16, v16, v98\n"
      "v_cvt_pk_bf16_f32 v176, v96, v97\n"
      "v_fma_f32 v101, v101, s54, -v14\n"
      "s_nop 1\n"
      "s_nop 0\n"
      "v_mfma_f32_32x32x16_bf16 v[112:127], a[92:95], a[28:31], v[112:127]\n"
      "ds_read_b128 a[124:127], v79 offset:49408\n"
      "s_waitcnt vmcnt(10)\n"
      "s_barrier\n"
      "s_nop 1\n"
      "s_waitcnt lgkmcnt(4)\n"
      "v_mfma_f32_32x32x16_bf16 v[112:127], a[96:99], a[32:35], v[112:127]\n"
      "ds_read_b128 a[64:67], v80 offset:0\n"
      "buffer_load_dwordx4 v8, s[44:47], s61 offen offset:2048 nt lds\n"
      "v_exp_f32 v100, v100\n"
      "v_add_f32 v17, v17, v99\n"
      "v_fma_f32 v102, v102, s54, -v14\n"
      "s_nop 1\n"
      "s_nop 0\n"
      "v_mfma_f32_32x32x16_bf16 v[112:127], a[100:103], a[36:39], v[112:127]\n"
      "ds_read_b128 a[68:71], v80 offset:16384\n"
      "v_exp_f32 v101, v101\n"
      "v_add_f32 v16, v16, v100\n"
      "v_cvt_pk_bf16_f32 v177, v98, v99\n"
      "v_fma_f32 v103, v103, s54, -v14\n"
      "v_exp_f32 v102, v102\n"
      "s_nop 1\n"
      "v_mfma_f32_32x32x16_bf16 v[112:127], a[104:107], a[40:43], v[112:127]\n"
      "ds_read_b128 a[72:75], v81 offset:0\n"
      "buffer_load_dwordx4 v9, s[44:47], s61 offen offset:3072 nt lds\n"
      "v_add_f32_e64 v17, v17, v101\n"
      "v_fma_f32 v104, v104, s54, -v14\n"
      "v_exp_f32_e64 v103, v103\n"
      "v_add_f32 v16, v16, v102\n"
      "s_nop 1\n"
      "v_mfma_f32_32x32x16_bf16 v[112:127], a[108:111], a[44:47], v[112:127]\n"
      "ds_read_b128 a[76:79], v81 offset:16384\n"
      "v_cvt_pk_bf16_f32 v178, v100, v101\n"
      "v_fma_f32 v105, v105, s54, -v14\n"
      "v_exp_f32 v104, v104\n"
      "v_add_f32 v17, v17, v103\n"
      "s_nop 1\n"
      "s_waitcnt lgkmcnt(4)\n"
      "v_mfma_f32_32x32x16_bf16 v[112:127], a[112:115], a[48:51], v[112:127]\n"
      "ds_read_b128 a[80:83], v82 offset:0\n"
      "v_fma_f32 v106, v106, s54, -v14\n"
      "v_exp_f32 v105, v105\n"
      "v_add_f32 v16, v16, v104\n"
      "v_cvt_pk_bf16_f32 v179, v102, v103\n"
      "v_fma_f32 v107, v107, s54, -v14\n"
      "v_exp_f32 v106, v106\n"
      "s_nop 1\n"
      "v_mfma_f32_32x32x16_bf16 v[112:127], a[116:119], a[52:55], v[112:127]\n"
      "ds_read_b128 a[84:87], v82 offset:16384\n"
      "s_add_u32 m0, s58, 49152\n"
      "s_mov_b32 s61, 196608\n"
      "v_add_f32_e64 v17, v17, v105\n"
      "v_fma_f32 v108, v108, s54, -v14\n"
      "v_exp_f32 v107, v107\n"
      "s_nop 1\n"
      "v_mfma_f32_32x32x16_bf16 v[112:127], a[120:123], a[56:59], v[112:127]\n"
      "ds_read_b128 a[88:91], v83 offset:0\n"
      "buffer_load_dwordx4 v2, s[40:43], s61 offen offset:0 nt lds\n"
      "v_add_f32_e64 v16, v16, v106\n"
      "v_cvt_pk_bf16_f32 v180, v104, v105\n"
      "v_fma_f32 v109, v109, s54, -v14\n"
      "v_exp_f32 v108, v108\n"
      "s_nop 1\n"
      "v_mfma_f32_32x32x16_bf16 v[112:127], a[124:127], a[60:63], v[112:127]\n"
      "ds_read_b128 a[92:95], v83 offset:16384\n"
      "v_add_f32_e64 v17, v17, v107\n"
      "v_fma_f32 v110, v110, s54, -v14\n"
      "v_exp_f32 v109, v109\n"
      "v_add_f32 v16, v16, v108\n"
      "s_nop 1\n"
      "s_waitcnt lgkmcnt(4)\n"
      "v_mfma_f32_32x32x16_bf16 v[32:47], a[64:67], a[0:3], 0\n"
      "ds_read_b128 a[96:99], v84 offset:0\n"
      "buffer_load_dwordx4 v3, s[40:43], s61 offen offset:1024 nt lds\n"
      "v_cvt_pk_bf16_f32 v181, v106, v107\n"
      "v_fma_f32 v111, v111, s54, -v14\n"
      "v_exp_f32 v110, v110\n"
      "s_nop 0\n"
      "v_mfma_f32_32x32x16_bf16 v[48:63], a[68:71], a[0:3], 0\n"
      "ds_read_b128 a[100:103], v84 offset:16384\n"
      "v_add_f32 v17, v17, v109\n"
      "v_exp_f32 v111, v111\n"
      "v_add_f32_e64 v16, v16, v110\n"
      "v_cvt_pk_bf16_f32 v182, v108, v109\n"
      "v_add_f32_e64 v17, v17, v111\n"
      "v_cvt_pk_bf16_f32 v183, v110, v111\n"
      "s_nop 1\n"
      "s_nop 0\n"
      "v_mfma_f32_32x32x16_bf16 v[32:47], a[72:75], a[4:7], v[32:47]\n"
      "ds_read_b128 a[104:107], v85 offset:0\n"
      "v_max3_f32 v18, v112, v113, v114\n"
      "v_max3_f32 v19, v119, v120, v121\n"
      "v_max3_f32 v18, v18, v115, v116\n"
      "v_max3_f32 v19, v19, v122, v123\n"
      "s_nop 0\n"
      "s_nop 0\n"
      "v_mfma_f32_32x32x16_bf16 v[48:63], a[76:79], a[4:7], v[48:63]\n"
      "ds_read_b128 a[108:111], v85 offset:16384\n"
      "buffer_load_dwordx4 v4, s[40:43], s61 offen offset:2048 nt lds\n"
      "v_max3_f32 v18, v18, v117, v118\n"
      "s_nop 1\n"
      "s_waitcnt lgkmcnt(4)\n"
      "v_mfma_f32_32x32x16_bf16 v[32:47], a[80:83], a[8:11], v[32:47]\n"
      "ds_read_b128 a[112:115], v86 offset:0\n"
      "v_max3_f32 v19, v19, v124, v125\n"
      "v_max3_f32 v19, v19, v126, v127\n"
      "v_max_f32 v18, v18, v19\n"
      "s_nop 0\n"
      "v_mfma_f32_32x32x16_bf16 v[48:63], a[84:87], a[8:11], v[48:63]\n"
      "ds_read_b128 a[116:119], v86 offset:16384\n"
      "v_cmp_gt_f32 vcc, v18, v15\n"
      "s_cbranch_vccnz .Lrares6_%=\n"
      ".Lrets6_%=:\n"
      "v_fma_f32 v112, v112, s54, -v14\n"
      "v_fma_f32 v113, v113, s54, -v14\n"
      "s_nop 1\n"
      "s_nop 0\n"
      "v_mfma_f32_32x32x16_bf16 v[32:47], a[88:91], a[12:15], v[32:47]\n"
      "ds_read_b128 a[120:123], v87 offset:0\n"
      "buffer_load_dwordx4 v5, s[40:43], s61 offen offset:3072 nt lds\n"
      "v_exp_f32 v112, v112\n"
      "s_nop 0\n"
      "v_mfma_f32_32x32x16_bf16 v[48:63], a[92:95], a[12:15], v[48:63]\n"
      "ds_read_b128 a[124:127], v87 offset:16384\n"
      "v_fma_f32 v114, v114, s54, -v14\n"
      "v_exp_f32_e64 v113, v113\n"
      "s_nop 1\n"
      "s_waitcnt lgkmcnt(4)\n"
      "v_mfma_f32_32x32x16_bf16 v[32:47], a[96:99], a[16:19], v[32:47]\n"
      "ds_read_b128 a[64:67], v80 offset:256\n"
      "v_add_f32_e64 v16, v16, v112\n"
      "v_fma_f32 v115, v115, s54, -v14\n"
      "v_exp_f32 v114, v114\n"
      "s_nop 0\n"
      "v_mfma_f32_32x32x16_bf16 v[48:63], a[100:103], a[16:19], v[48:63]\n"
      "ds_read_b128 a[68:71], v80 offset:16640\n"
      "s_add_u32 m0, s58, 65536\n"
      "s_mov_b32 s61, 212992\n"
      "v_add_f32 v17, v17, v113\n"
      "s_nop 1\n"
      "v_mfma_f32_32x32x16_bf16 v[32:47], a[104:107], a[20:23], v[32:47]\n"
      "ds_read_b128 a[72:75], v81 offset:256\n"
      "buffer_load_dwordx4 v2, s[40:43], s61 offen offset:0 nt lds\n"
      "v_fma_f32 v116, v116, s54, -v14\n"
      "v_exp_f32 v115, v115\n"
      "s_nop 0\n"
      "v_mfma_f32_32x32x16_bf16 v[48:63], a[108:111], a[20:23], v[48:63]\n"
      "ds_read_b128 a[76:79], v81 offset:16640\n"
      "v_add_f32_e64 v16, v16, v114\n"
      "v_cvt_pk_bf16_f32 v184, v112, v113\n"
      "s_nop 1\n"
      "s_waitcnt lgkmcnt(4)\n"
      "v_mfma_f32_32x32x16_bf16 v[32:47], a[112:115], a[24:27], v[32:47]\n"
      "ds_read_b128 a[80:83], v82 offset:256\n"
      "buffer_load_dwordx4 v3, s[40:43], s61 offen offset:1024 nt lds\n"
      "v_fma_f32 v117, v117, s54, -v14\n"
      "s_nop 0\n"
      "s_nop 0\n"
      "v_mfma_f32_32x32x16_bf16 v[48:63], a[116:119], a[24:27], v[48:63]\n"
      "ds_read_b128 a[84:87], v82 offset:16640\n"
      "v_exp_f32 v116, v116\n"
      "v_add_f32 v17, v17, v115\n"
      "v_fma_f32 v118, v118, s54, -v14\n"
      "s_nop 1\n"
      "s_nop 0\n"
      "v_mfma_f32_32x32x16_bf16 v[32:47], a[120:123], a[28:31], v[32:47]\n"
      "ds_read_b128 a[88:91], v83 offset:256\n"
      "v_exp_f32 v117, v117\n"
      "v_add_f32 v16, v16, v116\n"
      "v_cvt_pk_bf16_f32 v185, v114, v115\n"
      "s_nop 0\n"
      "s_nop 0\n"
      "v_mfma_f32_32x32x16_bf16 v[48:63], a[124:127], a[28:31], v[48:63]\n"
      "ds_read_b128 a[92:95], v83 offset:16640\n"
      "buffer_load_dwordx4 v4, s[40:43], s61 offen offset:2048 nt lds\n"
      "v_fma_f32 v119, v119, s54, -v14\n"
      "s_nop 1\n"
      "s_waitcnt lgkmcnt(4)\n"
      "v_mfma_f32_32x32x16_bf16 v[32:47], a[64:67], a[32:35], v[32:47]\n"
      "ds_read_b128 a[96:99], v84 offset:256\n"
      "v_exp_f32 v118, v118\n"
      "v_add_f32 v17, v17, v117\n"
      "v_fma_f32 v120, v120, s54, -v14\n"
      "s_nop 0\n"
      "s_nop 0\n"
      "v_mfma_f32_32x32x16_bf16 v[48:63], a[68:71], a[32:35], v[48:63]\n"
      "ds_read_b128 a[100:103], v84 offset:16640\n"
      "v_exp_f32 v119, v119\n"
      "v_add_f32 v16, v16, v118\n"
      "v_cvt_pk_bf16_f32 v186, v116, v117\n"
      "s_nop 0\n"
      "s_nop 0\n"
      "v_mfma_f32_32x32x16_bf16 v[32:47], a[72:75], a[36:39], v[32:47]\n"
      "ds_read_b128 a[104:107], v85 offset:256\n"
      "buffer_load_dwordx4 v5, s[40:43], s61 offen offset:3072 nt lds\n"
      "v_fma_f32 v121, v121, s54, -v14\n"
      "s_nop 0\n"
      "s_nop 0\n"
      "v_mfma_f32_32x32x16_bf16 v[48:63], a[76:79], a[36:39], v[48:63]\n"
      "ds_read_b128 a[108:111], v85 offset:16640\n"
      "v_exp_f32 v120, v120\n"
      "v_add_f32 v17, v17, v119\n"
      "s_nop 0\n"
      "s_waitcnt lgkmcnt(4)\n"
      "v_mfma_f32_32x32x16_bf16 v[32:47], a[80:83], a[40:43], v[32:47]\n"
      "ds_read_b128 a[112:115], v86 offset:256\n"
      "v_fma_f32 v122, v122, s54, -v14\n"
      "v_exp_f32_e64 v121, v121\n"
      "v_add_f32 v16, v16, v120\n"
      "s_nop 0\n"
      "v_mfma_f32_32x32x16_bf16 v[48:63], a[84:87], a[40:43], v[48:63]\n"
      "ds_read_b128 a[116:119], v86 offset:16640\n"
      "s_add_u32 m0, s58, 81920\n"
      "s_mov_b32 s61, 65536\n"
      "v_cvt_pk_bf16_f32 v187, v118, v119\n"
      "v_fma_f32 v123, v123, s54, -v14\n"
      "s_nop 0\n"
      "s_nop 0\n"
      "v_mfma_f32_32x32x16_bf16 v[32:47], a[88:91], a[44:47], v[32:47]\n"
      "ds_read_b128 a[120:123], v87 offset:256\n"
      "buffer_load_dwordx4 v6, s[44:47], s61 offen offset:0 nt lds\n"
      "v_exp_f32 v122, v122\n"
      "s_nop 0\n"
      "v_mfma_f32_32x32x16_bf16 v[48:63], a[92:95], a[44:47], v[48:63]\n"
      "ds_read_b128 a[124:127], v87 offset:16640\n"
      "v_add_f32_e64 v17, v17, v121\n"
      "v_fma_f32 v124, v124, s54, -v14\n"
      "s_nop 0\n"
      "s_waitcnt lgkmcnt(4)\n"
      "v_mfma_f32_32x32x16_bf16 v[32:47], a[96:99], a[48:51], v[32:47]\n"
      "v_exp_f32 v123, v123\n"
      "v_add_f32 v16, v16, v122\n"
      "v_cvt_pk_bf16_f32 v188, v120, v121\n"
      "v_fma_f32 v125, v125, s54, -v14\n"
      "s_nop 0\n"
      "s_nop 0\n"
      "v_mfma_f32_32x32x16_bf16 v[48:63], a[100:103], a[48:51], v[48:63]\n"
      "buffer_load_dwordx4 v7, s[44:47], s61 offen offset:1024 nt lds\n"
      "v_exp_f32_e64 v124, v124\n"
      "v_add_f32 v17, v17, v123\n"
      "s_nop 0\n"
      "v_mfma_f32_32x32x16_bf16 v[32:47], a[104:107], a[52:55], v[32:47]\n"
      "v_fma_f32 v126, v126, s54, -v14\n"
      "v_exp_f32 v125, v125\n"
      "v_add_f32 v16, v16, v124\n"
      "v_cvt_pk_bf16_f32 v189, v122, v123\n"
      "s_nop 0\n"
      "s_nop 0\n"
      "v_mfma_f32_32x32x16_bf16 v[48:63], a[108:111], a[52:55], v[48:63]\n"
      "s_waitcnt lgkmcnt(0)\n"
      "s_waitcnt vmcnt(10)\n"
      "s_barrier\n"
      "s_nop 0\n"
      "v_mfma_f32_32x32x16_bf16 v[32:47], a[112:115], a[56:59], v[32:47]\n"
      "ds_read_b128 a[64:67], v64 offset:0\n"
      "ds_read_b128 a[68:71], v64 offset:16384\n"
      "ds_read_b64_tr_b16 a[72:73], v10 offset:32768\n"
      "ds_read_b64_tr_b16 a[74:75], v10 offset:33792\n"
      "ds_read_b128 a[76:79], v65 offset:0\n"
      "ds_read_b128 a[80:83], v65 offset:16384\n"
      "buffer_load_dwordx4 v8, s[44:47], s61 offen offset:2048 nt lds\n"
      "s_nop 0\n"
      "s_nop 0\n"
      "v_mfma_f32_32x32x16_bf16 v[48:63], a[116:119], a[56:59], v[48:63]\n"
      "ds_read_b64_tr_b16 a[84:85], v11 offset:32768\n"
      "ds_read_b64_tr_b16 a[86:87], v11 offset:33792\n"
      "v_fma_f32 v127, v127, s54, -v14\n"
      "v_exp_f32 v126, v126\n"
      "s_nop 0\n"
      "v_mfma_f32_32x32x16_bf16 v[32:47], a[120:123], a[60:63], v[32:47]\n"
      "ds_read_b128 a[88:91], v66 offset:0\n"
      "v_add_f32_e64 v17, v17, v125\n"
      "v_exp_f32 v127, v127\n"
      "s_nop 0\n"
      "v_mfma_f32_32x32x16_bf16 v[48:63], a[124:127], a[60:63], v[48:63]\n"
      "ds_read_b128 a[92:95], v66 offset:16384\n"
      "buffer_load_dwordx4 v9, s[44:47], s61 offen offset:3072 nt lds\n"
      "v_add_f32 v16, v16, v126\n"
      "s_nop 0\n"
      "s_branch .Ljoin_%=\n"
      ".Lool_%=:\n"
      ".Lrares1_%=:\n"
      "s_swappc_b64 s[64:65], s[62:63]\n"
      "s_branch .Lrets1_%=\n"
      ".Lrares2_%=:\n"
      "s_swappc_b64 s[64:65], s[62:63]\n"
      "s_branch .Lrets2_%=\n"
      ".Lrares3_%=:\n"
      "s_swappc_b64 s[64:65], s[62:63]\n"
      "s_branch .Lrets3_%=\n"
      ".Lrares4_%=:\n"
      "s_swappc_b64 s[64:65], s[62:63]\n"
      "s_branch .Lrets4_%=\n"
      ".Lrares5_%=:\n"
      "s_swappc_b64 s[64:65], s[62:63]\n"
      "s_branch .Lrets5_%=\n"
      ".Lrares6_%=:\n"
      "s_swappc_b64 s[64:65], s[62:63]\n"
      "s_branch .Lrets6_%=\n"
      ".Lrare1_%=:\n"
      "s_swappc_b64 s[64:65], s[62:63]\n"
      "s_branch .Lret1_%=\n"
      ".Lrare2_%=:\n"
      "s_swappc_b64 s[64:65], s[62:63]\n"
      "s_branch .Lret2_%=\n"
      ".Lrare3_%=:\n"
      "s_swappc_b64 s[64:65], s[62:63]\n"
      "s_branch .Lret3_%=\n"
      ".Lrare4_%=:\n"
      "s_swappc_b64 s[64:65], s[62:63]\n"
      "s_branch .Lret4_%=\n"
      ".Lrare5_%=:\n"
      "s_swappc_b64 s[64:65], s[62:63]\n"
      "s_branch .Lret5_%=\n"
      ".Lrare6_%=:\n"
      "s_swappc_b64 s[64:65], s[62:63]\n"
      "s_branch .Lret6_%=\n"
      ".Lrare7_%=:\n"
      "s_swappc_b64 s[64:65], s[62:63]\n"
      "s_branch .Lret7_%=\n"
      ".Lrare8_%=:\n"
      "s_swappc_b64 s[64:65], s[62:63]\n"
      "s_branch .Lret8_%=\n"
      ".Lrare9_%=:\n"
      "s_swappc_b64 s[64:65], s[62:63]\n"
      "s_branch .Lret9_%=\n"
      ".Lrare10_%=:\n"
      "s_swappc_b64 s[64:65], s[62:63]\n"
      "v_mul_f32 v64, v26, v64\n"
      "v_mul_f32 v65, v26, v65\n"
      "v_mul_f32 v66, v26, v66\n"
      "v_mul_f32 v67, v26, v67\n"
      "v_mul_f32 v68, v26, v68\n"
      "v_mul_f32 v69, v26, v69\n"
      "v_mul_f32 v70, v26, v70\n"
      "v_mul_f32 v71, v26, v71\n"
      "v_mul_f32 v72, v26, v72\n"
      "v_mul_f32 v73, v26, v73\n"
      "v_mul_f32 v74, v26, v74\n"
      "v_mul_f32 v75, v26, v75\n"
      "v_mul_f32 v76, v26, v76\n"
      "v_mul_f32 v77, v26, v77\n"
      "v_mul_f32 v78, v26, v78\n"
      "v_mul_f32 v79, v26, v79\n"
      "v_mul_f32 v80, v26, v80\n"
      "v_mul_f32 v81, v26, v81\n"
      "v_mul_f32 v82, v26, v82\n"
      "v_mul_f32 v83, v26, v83\n"
      "v_mul_f32 v84, v26, v84\n"
      "v_mul_f32 v85, v26, v85\n"
      "v_mul_f32 v86, v26, v86\n"
      "v_mul_f32 v87, v26, v87\n"
      "v_mul_f32 v88, v26, v88\n"
      "v_mul_f32 v89, v26, v89\n"
      "v_mul_f32 v90, v26, v90\n"
      "v_mul_f32 v91, v26, v91\n"
      "v_mul_f32 v92, v26, v92\n"
      "v_mul_f32 v93, v26, v93\n"
      "v_mul_f32 v94, v26, v94\n"
      "v_mul_f32 v95, v26, v95\n"
      "s_nop 1\n"
      "s_branch .Lret10_%=\n"
      ".Lhandler_%=:\n"
      "v_mov_b32 v19, v18\n"
      "s_nop 1\n"
      "v_permlane32_swap_b32 v19, v18\n"
      "v_max3_f32 v25, v18, v19, v25\n"
      "v_mul_f32 v18, s54, v25\n"
      "v_sub_f32 v26, v14, v18\n"
      "v_exp_f32 v26, v26\n"
      "v_mov_b32 v14, v18\n"
      "v_add_f32 v15, s76, v25\n"
      "v_mul_f32 v16, v26, v16\n"
      "v_mul_f32 v17, v26, v17\n"
      "v_lshlrev_b32 v27, 16, v128\n"
      "v_and_b32 v28, 0xffff0000, v128\n"
      "v_mul_f32 v27, v26, v27\n"
      "v_mul_f32 v28, v26, v28\n"
      "v_cvt_pk_bf16_f32 v128, v27, v28\n"
      "v_lshlrev_b32 v27, 16, v129\n"
      "v_and_b32 v28, 0xffff0000, v129\n"
      "v_mul_f32 v27, v26, v27\n"
      "v_mul_f32 v28, v26, v28\n"
      "v_cvt_pk_bf16_f32 v129, v27, v28\n"
      "v_lshlrev_b32 v27, 16, v130\n"
      "v_and_b32 v28, 0xffff0000, v130\n"
      "v_mul_f32 v27, v26, v27\n"
      "v_mul_f32 v28, v26, v28\n"
      "v_cvt_pk_bf16_f32 v130, v27, v28\n"
      "v_lshlrev_b32 v27, 16, v131\n"
      "v_and_b32 v28, 0xffff0000, v131\n"
      "v_mul_f32 v27, v26, v27\n"
      "v_mul_f32 v28, v26, v28\n"
      "v_cvt_pk_bf16_f32 v131, v27, v28\n"
      "v_lshlrev_b32 v27, 16, v132\n"
      "v_and_b32 v28, 0xffff0000, v132\n"
      "v_mul_f32 v27, v26, v27\n"
      "v_mul_f32 v28, v26, v28\n"
      "v_cvt_pk_bf16_f32 v132, v27, v28\n"
      "v_lshlrev_b32 v27, 16, v133\n"
      "v_and_b32 v28, 0xffff0000, v133\n"
      "v_mul_f32 v27, v26, v27\n"
      "v_mul_f32 v28, v26, v28\n"
      "v_cvt_pk_bf16_f32 v133, v27, v28\n"
      "v_lshlrev_b32 v27, 16, v134\n"
      "v_and_b32 v28, 0xffff0000, v134\n"
      "v_mul_f32 v27, v26, v27\n"
      "v_mul_f32 v28, v26, v28\n"
      "v_cvt_pk_bf16_f32 v134, v27, v28\n"
      "v_lshlrev_b32 v27, 16, v135\n"
      "v_and_b32 v28, 0xffff0000, v135\n"
      "v_mul_f32 v27, v26, v27\n"
      "v_mul_f32 v28, v26, v28\n"
      "v_cvt_pk_bf16_f32 v135, v27, v28\n"
      "v_lshlrev_b32 v27, 16, v136\n"
      "v_and_b32 v28, 0xffff0000, v136\n"
      "v_mul_f32 v27, v26, v27\n"
      "v_mul_f32 v28, v26, v28\n"
      "v_cvt_pk_bf16_f32 v136, v27, v28\n"
      "v_lshlrev_b32 v27, 16, v137\n"
      "v_and_b32 v28, 0xffff0000, v137\n"
      "v_mul_f32 v27, v26, v27\n"
      "v_mul_f32 v28, v26, v28\n"
      "v_cvt_pk_bf16_f32 v137, v27, v28\n"
      "v_lshlrev_b32 v27, 16, v138\n"
      "v_and_b32 v28, 0xffff0000, v138\n"
      "v_mul_f32 v27, v26, v27\n"
      "v_mul_f32 v28, v26, v28\n"
      "v_cvt_pk_bf16_f32 v138, v27, v28\n"
      "v_lshlrev_b32 v27, 16, v139\n"
      "v_and_b32 v28, 0xffff0000, v139\n"
      "v_mul_f32 v27, v26, v27\n"
      "v_mul_f32 v28, v26, v28\n"
      "v_cvt_pk_bf16_f32 v139, v27, v28\n"
      "v_lshlrev_b32 v27, 16, v140\n"
      "v_and_b32 v28, 0xffff0000, v140\n"
      "v_mul_f32 v27, v26, v27\n"
      "v_mul_f32 v28, v26, v28\n"
      "v_cvt_pk_bf16_f32 v140, v27, v28\n"
      "v_lshlrev_b32 v27, 16, v141\n"
      "v_and_b32 v28, 0xffff0000, v141\n"
      "v_mul_f32 v27, v26, v27\n"
      "v_mul_f32 v28, v26, v28\n"
      "v_cvt_pk_bf16_f32 v141, v27, v28\n"
      "v_lshlrev_b32 v27, 16, v142\n"
      "v_and_b32 v28, 0xffff0000, v142\n"
      "v_mul_f32 v27, v26, v27\n"
      "v_mul_f32 v28, v26, v28\n"
      "v_cvt_pk_bf16_f32 v142, v27, v28\n"
      "v_lshlrev_b32 v27, 16, v143\n"
      "v_and_b32 v28, 0xffff0000, v143\n"
      "v_mul_f32 v27, v26, v27\n"
      "v_mul_f32 v28, v26, v28\n"
      "v_cvt_pk_bf16_f32 v143, v27, v28\n"
      "v_lshlrev_b32 v27, 16, v144\n"
      "v_and_b32 v28, 0xffff0000, v144\n"
      "v_mul_f32 v27, v26, v27\n"
      "v_mul_f32 v28, v26, v28\n"
      "v_cvt_pk_bf16_f32 v144, v27, v28\n"
      "v_lshlrev_b32 v27, 16, v145\n"
      "v_and_b32 v28, 0xffff0000, v145\n"
      "v_mul_f32 v27, v26, v27\n"
      "v_mul_f32 v28, v26, v28\n"
      "v_cvt_pk_bf16_f32 v145, v27, v28\n"
      "v_lshlrev_b32 v27, 16, v146\n"
      "v_and_b32 v28, 0xffff0000, v146\n"
      "v_mul_f32 v27, v26, v27\n"
      "v_mul_f32 v28, v26, v28\n"
      "v_cvt_pk_bf16_f32 v146, v27, v28\n"
      "v_lshlrev_b32 v27, 16, v147\n"
      "v_and_b32 v28, 0xffff0000, v147\n"
      "v_mul_f32 v27, v26, v27\n"
      "v_mul_f32 v28, v26, v28\n"
      "v_cvt_pk_bf16_f32 v147, v27, v28\n"
      "v_lshlrev_b32 v27, 16, v148\n"
      "v_and_b32 v28, 0xffff0000, v148\n"
      "v_mul_f32 v27, v26, v27\n"
      "v_mul_f32 v28, v26, v28\n"
      "v_cvt_pk_bf16_f32 v148, v27, v28\n"
      "v_lshlrev_b32 v27, 16, v149\n"
      "v_and_b32 v28, 0xffff0000, v149\n"
      "v_mul_f32 v27, v26, v27\n"
      "v_mul_f32 v28, v26, v28\n"
      "v_cvt_pk_bf16_f32 v149, v27, v28\n"
      "v_lshlrev_b32 v27, 16, v150\n"
      "v_and_b32 v28, 0xffff0000, v150\n"
      "v_mul_f32 v27, v26, v27\n"
      "v_mul_f32 v28, v26, v28\n"
      "v_cvt_pk_bf16_f32 v150, v27, v28\n"
      "v_lshlrev_b32 v27, 16, v151\n"
      "v_and_b32 v28, 0xffff0000, v151\n"
      "v_mul_f32 v27, v26, v27\n"
      "v_mul_f32 v28, v26, v28\n"
      "v_cvt_pk_bf16_f32 v151, v27, v28\n"
      "v_lshlrev_b32 v27, 16, v152\n"
      "v_and_b32 v28, 0xffff0000, v152\n"
      "v_mul_f32 v27, v26, v27\n"
      "v_mul_f32 v28, v26, v28\n"
      "v_cvt_pk_bf16_f32 v152, v27, v28\n"
      "v_lshlrev_b32 v27, 16, v153\n"
      "v_and_b32 v28, 0xffff0000, v153\n"
      "v_mul_f32 v27, v26, v27\n"
      "v_mul_f32 v28, v26, v28\n"
      "v_cvt_pk_bf16_f32 v153, v27, v28\n"
      "v_lshlrev_b32 v27, 16, v154\n"
      "v_and_b32 v28, 0xffff0000, v154\n"
      "v_mul_f32 v27, v26, v27\n"
      "v_mul_f32 v28, v26, v28\n"
      "v_cvt_pk_bf16_f32 v154, v27, v28\n"
      "v_lshlrev_b32 v27, 16, v155\n"
      "v_and_b32 v28, 0xffff0000, v155\n"
      "v_mul_f32 v27, v26, v27\n"
      "v_mul_f32 v28, v26, v28\n"
      "v_cvt_pk_bf16_f32 v155, v27, v28\n"
      "v_lshlrev_b32 v27, 16, v156\n"
      "v_and_b32 v28, 0xffff0000, v156\n"
      "v_mul_f32 v27, v26, v27\n"
      "v_mul_f32 v28, v26, v28\n"
      "v_cvt_pk_bf16_f32 v156, v27, v28\n"
      "v_lshlrev_b32 v27, 16, v157\n"
      "v_and_b32 v28, 0xffff0000, v157\n"
      "v_mul_f32 v27, v26, v27\n"
      "v_mul_f32 v28, v26, v28\n"
      "v_cvt_pk_bf16_f32 v157, v27, v28\n"
      "v_lshlrev_b32 v27, 16, v158\n"
      "v_and_b32 v28, 0xffff0000, v158\n"
      "v_mul_f32 v27, v26, v27\n"
      "v_mul_f32 v28, v26, v28\n"
      "v_cvt_pk_bf16_f32 v158, v27, v28\n"
      "v_lshlrev_b32 v27, 16, v159\n"
      "v_and_b32 v28, 0xffff0000, v159\n"
      "v_mul_f32 v27, v26, v27\n"
      "v_mul_f32 v28, v26, v28\n"
      "v_cvt_pk_bf16_f32 v159, v27, v28\n"
      "v_lshlrev_b32 v27, 16, v160\n"
      "v_and_b32 v28, 0xffff0000, v160\n"
      "v_mul_f32 v27, v26, v27\n"
      "v_mul_f32 v28, v26, v28\n"
      "v_cvt_pk_bf16_f32 v160, v27, v28\n"
      "v_lshlrev_b32 v27, 16, v161\n"
      "v_and_b32 v28, 0xffff0000, v161\n"
      "v_mul_f32 v27, v26, v27\n"
      "v_mul_f32 v28, v26, v28\n"
      "v_cvt_pk_bf16_f32 v161, v27, v28\n"
      "v_lshlrev_b32 v27, 16, v162\n"
      "v_and_b32 v28, 0xffff0000, v162\n"
      "v_mul_f32 v27, v26, v27\n"
      "v_mul_f32 v28, v26, v28\n"
      "v_cvt_pk_bf16_f32 v162, v27, v28\n"
      "v_lshlrev_b32 v27, 16, v163\n"
      "v_and_b32 v28, 0xffff0000, v163\n"
      "v_mul_f32 v27, v26, v27\n"
      "v_mul_f32 v28, v26, v28\n"
      "v_cvt_pk_bf16_f32 v163, v27, v28\n"
      "v_lshlrev_b32 v27, 16, v164\n"
      "v_and_b32 v28, 0xffff0000, v164\n"
      "v_mul_f32 v27, v26, v27\n"
      "v_mul_f32 v28, v26, v28\n"
      "v_cvt_pk_bf16_f32 v164, v27, v28\n"
      "v_lshlrev_b32 v27, 16, v165\n"
      "v_and_b32 v28, 0xffff0000, v165\n"
      "v_mul_f32 v27, v26, v27\n"
      "v_mul_f32 v28, v26, v28\n"
      "v_cvt_pk_bf16_f32 v165, v27, v28\n"
      "v_lshlrev_b32 v27, 16, v166\n"
      "v_and_b32 v28, 0xffff0000, v166\n"
      "v_mul_f32 v27, v26, v27\n"
      "v_mul_f32 v28, v26, v28\n"
      "v_cvt_pk_bf16_f32 v166, v27, v28\n"
      "v_lshlrev_b32 v27, 16, v167\n"
      "v_and_b32 v28, 0xffff0000, v167\n"
      "v_mul_f32 v27, v26, v27\n"
      "v_mul_f32 v28, v26, v28\n"
      "v_cvt_pk_bf16_f32 v167, v27, v28\n"
      "v_lshlrev_b32 v27, 16, v168\n"
      "v_and_b32 v28, 0xffff0000, v168\n"
      "v_mul_f32 v27, v26, v27\n"
      "v_mul_f32 v28, v26, v28\n"
      "v_cvt_pk_bf16_f32 v168, v27, v28\n"
      "v_lshlrev_b32 v27, 16, v169\n"
      "v_and_b32 v28, 0xffff0000, v169\n"
      "v_mul_f32 v27, v26, v27\n"
      "v_mul_f32 v28, v26, v28\n"
      "v_cvt_pk_bf16_f32 v169, v27, v28\n"
      "v_lshlrev_b32 v27, 16, v170\n"
      "v_and_b32 v28, 0xffff0000, v170\n"
      "v_mul_f32 v27, v26, v27\n"
      "v_mul_f32 v28, v26, v28\n"
      "v_cvt_pk_bf16_f32 v170, v27, v28\n"
      "v_lshlrev_b32 v27, 16, v171\n"
      "v_and_b32 v28, 0xffff0000, v171\n"
      "v_mul_f32 v27, v26, v27\n"
      "v_mul_f32 v28, v26, v28\n"
      "v_cvt_pk_bf16_f32 v171, v27, v28\n"
      "v_lshlrev_b32 v27, 16, v172\n"
      "v_and_b32 v28, 0xffff0000, v172\n"
      "v_mul_f32 v27, v26, v27\n"
      "v_mul_f32 v28, v26, v28\n"
      "v_cvt_pk_bf16_f32 v172, v27, v28\n"
      "v_lshlrev_b32 v27, 16, v173\n"
      "v_and_b32 v28, 0xffff0000, v173\n"
      "v_mul_f32 v27, v26, v27\n"
      "v_mul_f32 v28, v26, v28\n"
      "v_cvt_pk_bf16_f32 v173, v27, v28\n"
      "v_lshlrev_b32 v27, 16, v174\n"
      "v_and_b32 v28, 0xffff0000, v174\n"
      "v_mul_f32 v27, v26, v27\n"
      "v_mul_f32 v28, v26, v28\n"
      "v_cvt_pk_bf16_f32 v174, v27, v28\n"
      "v_lshlrev_b32 v27, 16, v175\n"
      "v_and_b32 v28, 0xffff0000, v175\n"
      "v_mul_f32 v27, v26, v27\n"
      "v_mul_f32 v28, v26, v28\n"
      "v_cvt_pk_bf16_f32 v175, v27, v28\n"
      "v_lshlrev_b32 v27, 16, v176\n"
      "v_and_b32 v28, 0xffff0000, v176\n"
      "v_mul_f32 v27, v26, v27\n"
      "v_mul_f32 v28, v26, v28\n"
      "v_cvt_pk_bf16_f32 v176, v27, v28\n"
      "v_lshlrev_b32 v27, 16, v177\n"
      "v_and_b32 v28, 0xffff0000, v177\n"
      "v_mul_f32 v27, v26, v27\n"
      "v_mul_f32 v28, v26, v28\n"
      "v_cvt_pk_bf16_f32 v177, v27, v28\n"
      "v_lshlrev_b32 v27, 16, v178\n"
      "v_and_b32 v28, 0xffff0000, v178\n"
      "v_mul_f32 v27, v26, v27\n"
      "v_mul_f32 v28, v26, v28\n"
      "v_cvt_pk_bf16_f32 v178, v27, v28\n"
      "v_lshlrev_b32 v27, 16, v179\n"
      "v_and_b32 v28, 0xffff0000, v179\n"
      "v_mul_f32 v27, v26, v27\n"
      "v_mul_f32 v28, v26, v28\n"
      "v_cvt_pk_bf16_f32 v179, v27, v28\n"
      "v_lshlrev_b32 v27, 16, v180\n"
      "v_and_b32 v28, 0xffff0000, v180\n"
      "v_mul_f32 v27, v26, v27\n"
      "v_mul_f32 v28, v26, v28\n"
      "v_cvt_pk_bf16_f32 v180, v27, v28\n"
      "v_lshlrev_b32 v27, 16, v181\n"
      "v_and_b32 v28, 0xffff0000, v181\n"
      "v_mul_f32 v27, v26, v27\n"
      "v_mul_f32 v28, v26, v28\n"
      "v_cvt_pk_bf16_f32 v181, v27, v28\n"
      "v_lshlrev_b32 v27, 16, v182\n"
      "v_and_b32 v28, 0xffff0000, v182\n"
      "v_mul_f32 v27, v26, v27\n"
      "v_mul_f32 v28, v26, v28\n"
      "v_cvt_pk_bf16_f32 v182, v27, v28\n"
      "v_lshlrev_b32 v27, 16, v183\n"
      "v_and_b32 v28, 0xffff0000, v183\n"
      "v_mul_f32 v27, v26, v27\n"
      "v_mul_f32 v28, v26, v28\n"
      "v_cvt_pk_bf16_f32 v183, v27, v28\n"
      "v_lshlrev_b32 v27, 16, v184\n"
      "v_and_b32 v28, 0xffff0000, v184\n"
      "v_mul_f32 v27, v26, v27\n"
      "v_mul_f32 v28, v26, v28\n"
      "v_cvt_pk_bf16_f32 v184, v27, v28\n"
      "v_lshlrev_b32 v27, 16, v185\n"
      "v_and_b32 v28, 0xffff0000, v185\n"
      "v_mul_f32 v27, v26, v27\n"
      "v_mul_f32 v28, v26, v28\n"
      "v_cvt_pk_bf16_f32 v185, v27, v28\n"
      "v_lshlrev_b32 v27, 16, v186\n"
      "v_and_b32 v28, 0xffff0000, v186\n"
      "v_mul_f32 v27, v26, v27\n"
      "v_mul_f32 v28, v26, v28\n"
      "v_cvt_pk_bf16_f32 v186, v27, v28\n"
      "v_lshlrev_b32 v27, 16, v187\n"
      "v_and_b32 v28, 0xffff0000, v187\n"
      "v_mul_f32 v27, v26, v27\n"
      "v_mul_f32 v28, v26, v28\n"
      "v_cvt_pk_bf16_f32 v187, v27, v28\n"
      "v_lshlrev_b32 v27, 16, v188\n"
      "v_and_b32 v28, 0xffff0000, v188\n"
      "v_mul_f32 v27, v26, v27\n"
      "v_mul_f32 v28, v26, v28\n"
      "v_cvt_pk_bf16_f32 v188, v27, v28\n"
      "v_lshlrev_b32 v27, 16, v189\n"
      "v_and_b32 v28, 0xffff0000, v189\n"
      "v_mul_f32 v27, v26, v27\n"
      "v_mul_f32 v28, v26, v28\n"
      "v_cvt_pk_bf16_f32 v189, v27, v28\n"
      "v_lshlrev_b32 v27, 16, v190\n"
      "v_and_b32 v28, 0xffff0000, v190\n"
      "v_mul_f32 v27, v26, v27\n"
      "v_mul_f32 v28, v26, v28\n"
      "v_cvt_pk_bf16_f32 v190, v27, v28\n"
      "v_lshlrev_b32 v27, 16, v191\n"
      "v_and_b32 v28, 0xffff0000, v191\n"
      "v_mul_f32 v27, v26, v27\n"
      "v_mul_f32 v28, v26, v28\n"
      "v_cvt_pk_bf16_f32 v191, v27, v28\n"
      "v_lshlrev_b32 v27, 16, v192\n"
      "v_and_b32 v28, 0xffff0000, v192\n"
      "v_mul_f32 v27, v26, v27\n"
      "v_mul_f32 v28, v26, v28\n"
      "v_cvt_pk_bf16_f32 v192, v27, v28\n"
      "v_lshlrev_b32 v27, 16, v193\n"
      "v_and_b32 v28, 0xffff0000, v193\n"
      "v_mul_f32 v27, v26, v27\n"
      "v_mul_f32 v28, v26, v28\n"
      "v_cvt_pk_bf16_f32 v193, v27, v28\n"
      "v_lshlrev_b32 v27, 16, v194\n"
      "v_and_b32 v28, 0xffff0000, v194\n"
      "v_mul_f32 v27, v26, v27\n"
      "v_mul_f32 v28, v26, v28\n"
      "v_cvt_pk_bf16_f32 v194, v27, v28\n"
      "v_lshlrev_b32 v27, 16, v195\n"
      "v_and_b32 v28, 0xffff0000, v195\n"
      "v_mul_f32 v27, v26, v27\n"
      "v_mul_f32 v28, v26, v28\n"
      "v_cvt_pk_bf16_f32 v195, v27, v28\n"
      "v_lshlrev_b32 v27, 16, v196\n"
      "v_and_b32 v28, 0xffff0000, v196\n"
      "v_mul_f32 v27, v26, v27\n"
      "v_mul_f32 v28, v26, v28\n"
      "v_cvt_pk_bf16_f32 v196, v27, v28\n"
      "v_lshlrev_b32 v27, 16, v197\n"
      "v_and_b32 v28, 0xffff0000, v197\n"
      "v_mul_f32 v27, v26, v27\n"
      "v_mul_f32 v28, v26, v28\n"
      "v_cvt_pk_bf16_f32 v197, v27, v28\n"
      "v_lshlrev_b32 v27, 16, v198\n"
      "v_and_b32 v28, 0xffff0000, v198\n"
      "v_mul_f32 v27, v26, v27\n"
      "v_mul_f32 v28, v26, v28\n"
      "v_cvt_pk_bf16_f32 v198, v27, v28\n"
      "v_lshlrev_b32 v27, 16, v199\n"
      "v_and_b32 v28, 0xffff0000, v199\n"
      "v_mul_f32 v27, v26, v27\n"
      "v_mul_f32 v28, v26, v28\n"
      "v_cvt_pk_bf16_f32 v199, v27, v28\n"
      "v_lshlrev_b32 v27, 16, v200\n"
      "v_and_b32 v28, 0xffff0000, v200\n"
      "v_mul_f32 v27, v26, v27\n"
      "v_mul_f32 v28, v26, v28\n"
      "v_cvt_pk_bf16_f32 v200, v27, v28\n"
      "v_lshlrev_b32 v27, 16, v201\n"
      "v_and_b32 v28, 0xffff0000, v201\n"
      "v_mul_f32 v27, v26, v27\n"
      "v_mul_f32 v28, v26, v28\n"
      "v_cvt_pk_bf16_f32 v201, v27, v28\n"
      "v_lshlrev_b32 v27, 16, v202\n"
      "v_and_b32 v28, 0xffff0000, v202\n"
      "v_mul_f32 v27, v26, v27\n"
      "v_mul_f32 v28, v26, v28\n"
      "v_cvt_pk_bf16_f32 v202, v27, v28\n"
      "v_lshlrev_b32 v27, 16, v203\n"
      "v_and_b32 v28, 0xffff0000, v203\n"
      "v_mul_f32 v27, v26, v27\n"
      "v_mul_f32 v28, v26, v28\n"
      "v_cvt_pk_bf16_f32 v203, v27, v28\n"
      "v_lshlrev_b32 v27, 16, v204\n"
      "v_and_b32 v28, 0xffff0000, v204\n"
      "v_mul_f32 v27, v26, v27\n"
      "v_mul_f32 v28, v26, v28\n"
      "v_cvt_pk_bf16_f32 v204, v27, v28\n"
      "v_lshlrev_b32 v27, 16, v205\n"
      "v_and_b32 v28, 0xffff0000, v205\n"
      "v_mul_f32 v27, v26, v27\n"
      "v_mul_f32 v28, v26, v28\n"
      "v_cvt_pk_bf16_f32 v205, v27, v28\n"
      "v_lshlrev_b32 v27, 16, v206\n"
      "v_and_b32 v28, 0xffff0000, v206\n"
      "v_mul_f32 v27, v26, v27\n"
      "v_mul_f32 v28, v26, v28\n"
      "v_cvt_pk_bf16_f32 v206, v27, v28\n"
      "v_lshlrev_b32 v27, 16, v207\n"
      "v_and_b32 v28, 0xffff0000, v207\n"
      "v_mul_f32 v27, v26, v27\n"
      "v_mul_f32 v28, v26, v28\n"
      "v_cvt_pk_bf16_f32 v207, v27, v28\n"
      "v_lshlrev_b32 v27, 16, v208\n"
      "v_and_b32 v28, 0xffff0000, v208\n"
      "v_mul_f32 v27, v26, v27\n"
      "v_mul_f32 v28, v26, v28\n"
      "v_cvt_pk_bf16_f32 v208, v27, v28\n"
      "v_lshlrev_b32 v27, 16, v209\n"
      "v_and_b32 v28, 0xffff0000, v209\n"
      "v_mul_f32 v27, v26, v27\n"
      "v_mul_f32 v28, v26, v28\n"
      "v_cvt_pk_bf16_f32 v209, v27, v28\n"
      "v_lshlrev_b32 v27, 16, v210\n"
      "v_and_b32 v28, 0xffff0000, v210\n"
      "v_mul_f32 v27, v26, v27\n"
      "v_mul_f32 v28, v26, v28\n"
      "v_cvt_pk_bf16_f32 v210, v27, v28\n"
      "v_lshlrev_b32 v27, 16, v211\n"
      "v_and_b32 v28, 0xffff0000, v211\n"
      "v_mul_f32 v27, v26, v27\n"
      "v_mul_f32 v28, v26, v28\n"
      "v_cvt_pk_bf16_f32 v211, v27, v28\n"
      "v_lshlrev_b32 v27, 16, v212\n"
      "v_and_b32 v28, 0xffff0000, v212\n"
      "v_mul_f32 v27, v26, v27\n"
      "v_mul_f32 v28, v26, v28\n"
      "v_cvt_pk_bf16_f32 v212, v27, v28\n"
      "v_lshlrev_b32 v27, 16, v213\n"
      "v_and_b32 v28, 0xffff0000, v213\n"
      "v_mul_f32 v27, v26, v27\n"
      "v_mul_f32 v28, v26, v28\n"
      "v_cvt_pk_bf16_f32 v213, v27, v28\n"
      "v_lshlrev_b32 v27, 16, v214\n"
      "v_and_b32 v28, 0xffff0000, v214\n"
      "v_mul_f32 v27, v26, v27\n"
      "v_mul_f32 v28, v26, v28\n"
      "v_cvt_pk_bf16_f32 v214, v27, v28\n"
      "v_lshlrev_b32 v27, 16, v215\n"
      "v_and_b32 v28, 0xffff0000, v215\n"
      "v_mul_f32 v27, v26, v27\n"
      "v_mul_f32 v28, v26, v28\n"
      "v_cvt_pk_bf16_f32 v215, v27, v28\n"
      "v_lshlrev_b32 v27, 16, v216\n"
      "v_and_b32 v28, 0xffff0000, v216\n"
      "v_mul_f32 v27, v26, v27\n"
      "v_mul_f32 v28, v26, v28\n"
      "v_cvt_pk_bf16_f32 v216, v27, v28\n"
      "v_lshlrev_b32 v27, 16, v217\n"
      "v_and_b32 v28, 0xffff0000, v217\n"
      "v_mul_f32 v27, v26, v27\n"
      "v_mul_f32 v28, v26, v28\n"
      "v_cvt_pk_bf16_f32 v217, v27, v28\n"
      "v_lshlrev_b32 v27, 16, v218\n"
      "v_and_b32 v28, 0xffff0000, v218\n"
      "v_mul_f32 v27, v26, v27\n"
      "v_mul_f32 v28, v26, v28\n"
      "v_cvt_pk_bf16_f32 v218, v27, v28\n"
      "v_lshlrev_b32 v27, 16, v219\n"
      "v_and_b32 v28, 0xffff0000, v219\n"
      "v_mul_f32 v27, v26, v27\n"
      "v_mul_f32 v28, v26, v28\n"
      "v_cvt_pk_bf16_f32 v219, v27, v28\n"
      "v_lshlrev_b32 v27, 16, v220\n"
      "v_and_b32 v28, 0xffff0000, v220\n"
      "v_mul_f32 v27, v26, v27\n"
      "v_mul_f32 v28, v26, v28\n"
      "v_cvt_pk_bf16_f32 v220, v27, v28\n"
      "v_lshlrev_b32 v27, 16, v221\n"
      "v_and_b32 v28, 0xffff0000, v221\n"
      "v_mul_f32 v27, v26, v27\n"
      "v_mul_f32 v28, v26, v28\n"
      "v_cvt_pk_bf16_f32 v221, v27, v28\n"
      "v_lshlrev_b32 v27, 16, v222\n"
      "v_and_b32 v28, 0xffff0000, v222\n"
      "v_mul_f32 v27, v26, v27\n"
      "v_mul_f32 v28, v26, v28\n"
      "v_cvt_pk_bf16_f32 v222, v27, v28\n"
      "v_lshlrev_b32 v27, 16, v223\n"
      "v_and_b32 v28, 0xffff0000, v223\n"
      "v_mul_f32 v27, v26, v27\n"
      "v_mul_f32 v28, v26, v28\n"
      "v_cvt_pk_bf16_f32 v223, v27, v28\n"
      "v_lshlrev_b32 v27, 16, v224\n"
      "v_and_b32 v28, 0xffff0000, v224\n"
      "v_mul_f32 v27, v26, v27\n"
      "v_mul_f32 v28, v26, v28\n"
      "v_cvt_pk_bf16_f32 v224, v27, v28\n"
      "v_lshlrev_b32 v27, 16, v225\n"
      "v_and_b32 v28, 0xffff0000, v225\n"
      "v_mul_f32 v27, v26, v27\n"
      "v_mul_f32 v28, v26, v28\n"
      "v_cvt_pk_bf16_f32 v225, v27, v28\n"
      "v_lshlrev_b32 v27, 16, v226\n"
      "v_and_b32 v28, 0xffff0000, v226\n"
      "v_mul_f32 v27, v26, v27\n"
      "v_mul_f32 v28, v26, v28\n"
      "v_cvt_pk_bf16_f32 v226, v27, v28\n"
      "v_lshlrev_b32 v27, 16, v227\n"
      "v_and_b32 v28, 0xffff0000, v227\n"
      "v_mul_f32 v27, v26, v27\n"
      "v_mul_f32 v28, v26, v28\n"
      "v_cvt_pk_bf16_f32 v227, v27, v28\n"
      "v_lshlrev_b32 v27, 16, v228\n"
      "v_and_b32 v28, 0xffff0000, v228\n"
      "v_mul_f32 v27, v26, v27\n"
      "v_mul_f32 v28, v26, v28\n"
      "v_cvt_pk_bf16_f32 v228, v27, v28\n"
      "v_lshlrev_b32 v27, 16, v229\n"
      "v_and_b32 v28, 0xffff0000, v229\n"
      "v_mul_f32 v27, v26, v27\n"
      "v_mul_f32 v28, v26, v28\n"
      "v_cvt_pk_bf16_f32 v229, v27, v28\n"
      "v_lshlrev_b32 v27, 16, v230\n"
      "v_and_b32 v28, 0xffff0000, v230\n"
      "v_mul_f32 v27, v26, v27\n"
      "v_mul_f32 v28, v26, v28\n"
      "v_cvt_pk_bf16_f32 v230, v27, v28\n"
      "v_lshlrev_b32 v27, 16, v231\n"
      "v_and_b32 v28, 0xffff0000, v231\n"
      "v_mul_f32 v27, v26, v27\n"
      "v_mul_f32 v28, v26, v28\n"
      "v_cvt_pk_bf16_f32 v231, v27, v28\n"
      "v_lshlrev_b32 v27, 16, v232\n"
      "v_and_b32 v28, 0xffff0000, v232\n"
      "v_mul_f32 v27, v26, v27\n"
      "v_mul_f32 v28, v26, v28\n"
      "v_cvt_pk_bf16_f32 v232, v27, v28\n"
      "v_lshlrev_b32 v27, 16, v233\n"
      "v_and_b32 v28, 0xffff0000, v233\n"
      "v_mul_f32 v27, v26, v27\n"
      "v_mul_f32 v28, v26, v28\n"
      "v_cvt_pk_bf16_f32 v233, v27, v28\n"
      "v_lshlrev_b32 v27, 16, v234\n"
      "v_and_b32 v28, 0xffff0000, v234\n"
      "v_mul_f32 v27, v26, v27\n"
      "v_mul_f32 v28, v26, v28\n"
      "v_cvt_pk_bf16_f32 v234, v27, v28\n"
      "v_lshlrev_b32 v27, 16, v235\n"
      "v_and_b32 v28, 0xffff0000, v235\n"
      "v_mul_f32 v27, v26, v27\n"
      "v_mul_f32 v28, v26, v28\n"
      "v_cvt_pk_bf16_f32 v235, v27, v28\n"
      "v_lshlrev_b32 v27, 16, v236\n"
      "v_and_b32 v28, 0xffff0000, v236\n"
      "v_mul_f32 v27, v26, v27\n"
      "v_mul_f32 v28, v26, v28\n"
      "v_cvt_pk_bf16_f32 v236, v27, v28\n"
      "v_lshlrev_b32 v27, 16, v237\n"
      "v_and_b32 v28, 0xffff0000, v237\n"
      "v_mul_f32 v27, v26, v27\n"
      "v_mul_f32 v28, v26, v28\n"
      "v_cvt_pk_bf16_f32 v237, v27, v28\n"
      "v_lshlrev_b32 v27, 16, v238\n"
      "v_and_b32 v28, 0xffff0000, v238\n"
      "v_mul_f32 v27, v26, v27\n"
      "v_mul_f32 v28, v26, v28\n"
      "v_cvt_pk_bf16_f32 v238, v27, v28\n"
      "v_lshlrev_b32 v27, 16, v239\n"
      "v_and_b32 v28, 0xffff0000, v239\n"
      "v_mul_f32 v27, v26, v27\n"
      "v_mul_f32 v28, v26, v28\n"
      "v_cvt_pk_bf16_f32 v239, v27, v28\n"
      "v_accvgpr_read_b32 v27, a128\n"
      "v_mul_f32 v27, v26, v27\n"
      "v_accvgpr_write_b32 a128, v27\n"
      "v_accvgpr_read_b32 v27, a129\n"
      "v_mul_f32 v27, v26, v27\n"
      "v_accvgpr_write_b32 a129, v27\n"
      "v_accvgpr_read_b32 v27, a130\n"
      "v_mul_f32 v27, v26, v27\n"
      "v_accvgpr_write_b32 a130, v27\n"
      "v_accvgpr_read_b32 v27, a131\n"
      "v_mul_f32 v27, v26, v27\n"
      "v_accvgpr_write_b32 a131, v27\n"
      "v_accvgpr_read_b32 v27, a132\n"
      "v_mul_f32 v27, v26, v27\n"
      "v_accvgpr_write_b32 a132, v27\n"
      "v_accvgpr_read_b32 v27, a133\n"
      "v_mul_f32 v27, v26, v27\n"
      "v_accvgpr_write_b32 a133, v27\n"
      "v_accvgpr_read_b32 v27, a134\n"
      "v_mul_f32 v27, v26, v27\n"
      "v_accvgpr_write_b32 a134, v27\n"
      "v_accvgpr_read_b32 v27, a135\n"
      "v_mul_f32 v27, v26, v27\n"
      "v_accvgpr_write_b32 a135, v27\n"
      "v_accvgpr_read_b32 v27, a136\n"
      "v_mul_f32 v27, v26, v27\n"
      "v_accvgpr_write_b32 a136, v27\n"
      "v_accvgpr_read_b32 v27, a137\n"
      "v_mul_f32 v27, v26, v27\n"
      "v_accvgpr_write_b32 a137, v27\n"
      "v_accvgpr_read_b32 v27, a138\n"
      "v_mul_f32 v27, v26, v27\n"
      "v_accvgpr_write_b32 a138, v27\n"
      "v_accvgpr_read_b32 v27, a139\n"
      "v_mul_f32 v27, v26, v27\n"
      "v_accvgpr_write_b32 a139, v27\n"
      "v_accvgpr_read_b32 v27, a140\n"
      "v_mul_f32 v27, v26, v27\n"
      "v_accvgpr_write_b32 a140, v27\n"
      "v_accvgpr_read_b32 v27, a141\n"
      "v_mul_f32 v27, v26, v27\n"
      "v_accvgpr_write_b32 a141, v27\n"
      "v_accvgpr_read_b32 v27, a142\n"
      "v_mul_f32 v27, v26, v27\n"
      "v_accvgpr_write_b32 a142, v27\n"
      "v_accvgpr_read_b32 v27, a143\n"
      "v_mul_f32 v27, v26, v27\n"
      "v_accvgpr_write_b32 a143, v27\n"
      "v_accvgpr_read_b32 v27, a144\n"
      "v_mul_f32 v27, v26, v27\n"
      "v_accvgpr_write_b32 a144, v27\n"
      "v_accvgpr_read_b32 v27, a145\n"
      "v_mul_f32 v27, v26, v27\n"
      "v_accvgpr_write_b32 a145, v27\n"
      "v_accvgpr_read_b32 v27, a146\n"
      "v_mul_f32 v27, v26, v27\n"
      "v_accvgpr_write_b32 a146, v27\n"
      "v_accvgpr_read_b32 v27, a147\n"
      "v_mul_f32 v27, v26, v27\n"
      "v_accvgpr_write_b32 a147, v27\n"
      "v_accvgpr_read_b32 v27, a148\n"
      "v_mul_f32 v27, v26, v27\n"
      "v_accvgpr_write_b32 a148, v27\n"
      "v_accvgpr_read_b32 v27, a149\n"
      "v_mul_f32 v27, v26, v27\n"
      "v_accvgpr_write_b32 a149, v27\n"
      "v_accvgpr_read_b32 v27, a150\n"
      "v_mul_f32 v27, v26, v27\n"
      "v_accvgpr_write_b32 a150, v27\n"
      "v_accvgpr_read_b32 v27, a151\n"
      "v_mul_f32 v27, v26, v27\n"
      "v_accvgpr_write_b32 a151, v27\n"
      "v_accvgpr_read_b32 v27, a152\n"
      "v_mul_f32 v27, v26, v27\n"
      "v_accvgpr_write_b32 a152, v27\n"
      "v_accvgpr_read_b32 v27, a153\n"
      "v_mul_f32 v27, v26, v27\n"
      "v_accvgpr_write_b32 a153, v27\n"
      "v_accvgpr_read_b32 v27, a154\n"
      "v_mul_f32 v27, v26, v27\n"
      "v_accvgpr_write_b32 a154, v27\n"
      "v_accvgpr_read_b32 v27, a155\n"
      "v_mul_f32 v27, v26, v27\n"
      "v_accvgpr_write_b32 a155, v27\n"
      "v_accvgpr_read_b32 v27, a156\n"
      "v_mul_f32 v27, v26, v27\n"
      "v_accvgpr_write_b32 a156, v27\n"
      "v_accvgpr_read_b32 v27, a157\n"
      "v_mul_f32 v27, v26, v27\n"
      "v_accvgpr_write_b32 a157, v27\n"
      "v_accvgpr_read_b32 v27, a158\n"
      "v_mul_f32 v27, v26, v27\n"
      "v_accvgpr_write_b32 a158, v27\n"
      "v_accvgpr_read_b32 v27, a159\n"
      "v_mul_f32 v27, v26, v27\n"
      "v_accvgpr_write_b32 a159, v27\n"
      "s_nop 1\n"
      "s_setpc_b64 s[64:65]\n"
      ".Lend_%=:\n"
      :
      : "s"(__builtin_amdgcn_kernarg_segment_ptr()), "s"(blockIdx.x), "v"(threadIdx.x), "s"((unsigned)(size_t)lds)
      : "memory", "vcc", "scc", "v1", "v2", "v3", "v4", "v5", "v6", "v7", "v8", "v9", "v10", "v11", "v12", "v13", "v14", "v15", "v16",
        "v17", "v18", "v19", "v20", "v21", "v22", "v23", "v24", "v25", "v26", "v27", "v28", "v29", "v30", "v31", "v32",
        "v33", "v34", "v35", "v36", "v37", "v38", "v39", "v40", "v41", "v42", "v43", "v44", "v45", "v46", "v47", "v48",
        "v49", "v50", "v51", "v52", "v53", "v54", "v55", "v56", "v57", "v58", "v59", "v60", "v61", "v62", "v63", "v64",
        "v65", "v66", "v67", "v68", "v69", "v70", "v71", "v72", "v73", "v74", "v75", "v76", "v77", "v78", "v79", "v80",
        "v81", "v82", "v83", "v84", "v85", "v86", "v87", "v88", "v89", "v90", "v91", "v92", "v93", "v94", "v95", "v96",
        "v97", "v98", "v99", "v100", "v101", "v102", "v103", "v104", "v105", "v106", "v107", "v108", "v109", "v110", "v111", "v112",
        "v113", "v114", "v115", "v116", "v117", "v118", "v119", "v120", "v121", "v122", "v123", "v124", "v125", "v126", "v127", "v128",
        "v129", "v130", "v131", "v132", "v133", "v134", "v135", "v136", "v137", "v138", "v139", "v140", "v141", "v142", "v143", "v144",
        "v145", "v146", "v147", "v148", "v149", "v150", "v151", "v152", "v153", "v154", "v155", "v156", "v157", "v158", "v159", "v160",
        "v161", "v162", "v163", "v164", "v165", "v166", "v167", "v168", "v169", "v170", "v171", "v172", "v173", "v174", "v175", "v176",
        "v177", "v178", "v179", "v180", "v181", "v182", "v183", "v184", "v185", "v186", "v187", "v188", "v189", "v190", "v191", "v192",
        "v193", "v194", "v195", "v196", "v197", "v198", "v199", "v200", "v201", "v202", "v203", "v204", "v205", "v206", "v207", "v208",
        "v209", "v210", "v211", "v212", "v213", "v214", "v215", "v216", "v217", "v218", "v219", "v220", "v221", "v222", "v223", "v224",
        "v225", "v226", "v227", "v228", "v229", "v230", "v231", "v232", "v233", "v234", "v235", "v236", "v237", "v238", "v239", "v240",
        "v241", "v242", "v243", "v244", "v245", "v246", "v247", "v248", "v249", "v250", "v251", "v252", "v253", "v254", "v255", "a0",
        "a1", "a2", "a3", "a4", "a5", "a6", "a7", "a8", "a9", "a10", "a11", "a12", "a13", "a14", "a15", "a16",
        "a17", "a18", "a19", "a20", "a21", "a22", "a23", "a24", "a25", "a26", "a27", "a28", "a29", "a30", "a31", "a32",
        "a33", "a34", "a35", "a36", "a37", "a38", "a39", "a40", "a41", "a42", "a43", "a44", "a45", "a46", "a47", "a48",
        "a49", "a50", "a51", "a52", "a53", "a54", "a55", "a56", "a57", "a58", "a59", "a60", "a61", "a62", "a63", "a64",
        "a65", "a66", "a67", "a68", "a69", "a70", "a71", "a72", "a73", "a74", "a75", "a76", "a77", "a78", "a79", "a80",
        "a81", "a82", "a83", "a84", "a85", "a86", "a87", "a88", "a89", "a90", "a91", "a92", "a93", "a94", "a95", "a96",
        "a97", "a98", "a99", "a100", "a101", "a102", "a103", "a104", "a105", "a106", "a107", "a108", "a109", "a110", "a111", "a112",
        "a113", "a114", "a115", "a116", "a117", "a118", "a119", "a120", "a121", "a122", "a123", "a124", "a125", "a126", "a127", "a128",
        "a129", "a130", "a131", "a132", "a133", "a134", "a135", "a136", "a137", "a138", "a139", "a140", "a141", "a142", "a143", "a144",
        "a145", "a146", "a147", "a148", "a149", "a150", "a151", "a152", "a153", "a154", "a155", "a156", "a157", "a158", "a159", "s36",
        "s37", "s38", "s39", "s40", "s41", "s42", "s43", "s44", "s45", "s46", "s47", "s48", "s49", "s50", "s51", "s52",
        "s53", "s54", "s55", "s56", "s57", "s58", "s59", "s60", "s61", "s62", "s63", "s64", "s65", "s66", "s67", "s68",
        "s69", "s70", "s71", "s72", "s73", "s74", "s75", "s76", "s77", "s78", "s79", "s80", "s81", "s82", "s83", "s84",
        "s85", "s86", "s87", "s88", "s89", "s90", "s91", "s92", "s93", "s94", "s95", "s96", "s97", "s98", "s99");
}

extern "C" void kernel_launch(void* const* d_in, const int* in_sizes, int n_in, void* d_out, int out_size, void* d_ws, size_t ws_size,
                              hipStream_t stream) {
  mha_fwd<<<256, 256, 0, stream>>>(d_in[0], d_in[1], d_in[2], d_out);
}
```
